```python
import torch

def _softmax(x, axis=-1):
    return torch.softmax(x, dim=axis)
def _gelu(x):
    return torch.nn.functional.gelu(x, approximate="tanh")
def _silu(x):
    return torch.nn.functional.silu(x)
def _sigmoid(x):
    return torch.sigmoid(x)
def _layernorm(x, axis=-1, eps=1e-5):
    m = x.mean(dim=axis, keepdim=True)
    v = x.var(dim=axis, keepdim=True, unbiased=False)
    return (x - m) / torch.sqrt(v + eps)
def _scatter_add(tgt, idx, src, axis=0):
    return tgt.clone().index_add_(axis, idx, src)
def _segment_sum(src, seg, n):
    out = torch.zeros((n,) + src.shape[1:], dtype=src.dtype)
    return out.index_add_(0, seg, src)
def _pad(x, pw):
    flat = tuple(v for pair in reversed(pw) for v in pair)
    return torch.nn.functional.pad(x, flat)

kernel_name = 'sequential_preLN__sliding_block_cached__geglu_ffn__decode'

def setup_inputs():

    _w = (lambda a: (lambda t: t.to(torch.bfloat16) if t.is_floating_point() else t)(torch.as_tensor(a)))
    def _A_setup():
        B, Sq, D = 2, 1, 640
        Hq, Hkv, Sk, Dh = 32, 32, 8192, 192
        assert Hq % Hkv == 0, (Hq, Hkv)
        return {
            "x": _w((lambda *s: torch.randn(*s, dtype=torch.bfloat16) * 0.05)(B, Sq, D)),
            "k_cache": _w((lambda *s: torch.randn(*s, dtype=torch.bfloat16) * 3.2)(B, Hkv, Sk, Dh)),
            "v_cache": _w((lambda *s: torch.randn(*s, dtype=torch.bfloat16) * 0.4)(B, Hkv, Sk, Dh)),
            "wq": _w((lambda *s: torch.randn(*s, dtype=torch.bfloat16) * 3.2)(D, Hq * Dh) * (D ** -0.5)),
            "wo": _w((lambda *s: torch.randn(*s, dtype=torch.bfloat16) * 0.4)(Hq * Dh, D) * ((Hq * Dh) ** -0.5)),
        }
    _A_in = _A_setup()

    def _B_setup():
        B, S, D, F = 2, 1, 640, 1536
        return {
            "x": _w((lambda *s: torch.randn(*s, dtype=torch.bfloat16) * 0.05)(B, S, D)),
            "w_gate": _w((lambda *s: torch.randn(*s, dtype=torch.bfloat16) * 0.05)(D, F)),
            "w_up": _w((lambda *s: torch.randn(*s, dtype=torch.bfloat16) * 0.05)(D, F)),
            "w_down": _w((lambda *s: torch.randn(*s, dtype=torch.bfloat16) * 0.05)(F, D)),
        }
    _B_in = _B_setup()

    return {
        "A_x": _A_in["x"],
        "A_k_cache": _A_in["k_cache"],
        "A_v_cache": _A_in["v_cache"],
        "A_wq": _A_in["wq"],
        "A_wo": _A_in["wo"],
        "B_x": _B_in["x"],
        "B_w_gate": _B_in["w_gate"],
        "B_w_up": _B_in["w_up"],
        "B_w_down": _B_in["w_down"],
    }

def reference(A_x, A_k_cache, A_v_cache, A_wq, A_wo, B_x, B_w_gate, B_w_up, B_w_down):
    def _A_ref(x, k_cache, v_cache, wq, wo):
        B, Sq, D = x.shape
        Hq, Dh = 32, 192
        Hkv, Sk = k_cache.shape[1], k_cache.shape[2]
        W = min(4096, max(256, Sk // 4))
        rep = Hq // Hkv
        q = (lambda x, *d: x.permute(*d))((x @ wq).reshape(B, Sq, Hq, Dh), 0, 2, 1, 3)
        k = (lambda x, n, axis: torch.repeat_interleave(x, n, dim=axis))(k_cache[:, :, Sk - W:, :], rep, axis=1)
        v = (lambda x, n, axis: torch.repeat_interleave(x, n, dim=axis))(v_cache[:, :, Sk - W:, :], rep, axis=1)
        scores = torch.einsum("bhqd,bhkd->bhqk", q, k) * (Dh ** -0.5)
        p = _softmax(scores, axis=-1)
        o = torch.einsum("bhqk,bhkd->bhqd", p, v)
        return (lambda x, *d: x.permute(*d))(o, 0, 2, 1, 3).reshape(B, Sq, Hq * Dh) @ wo

    def _B_ref(x, w_gate, w_up, w_down):
        return (_gelu(x @ w_gate) * (x @ w_up)) @ w_down

    x = A_x
    h = x + _A_ref(_layernorm(x, axis=-1), A_k_cache, A_v_cache, A_wq, A_wo)
    h2 = h + _B_ref(_layernorm(h, axis=-1), B_w_gate, B_w_up, B_w_down)
    return h2
```

```cpp
#include <hip/hip_runtime.h>
#include <hip/hip_bf16.h>
#include <stdint.h>

#define GAS __attribute__((address_space(1)))
typedef unsigned v4u __attribute__((ext_vector_type(4)));
typedef float f32x4 __attribute__((ext_vector_type(4)));
typedef float f32x2 __attribute__((ext_vector_type(2)));
typedef __bf16 bf16x2 __attribute__((ext_vector_type(2)));
typedef GAS unsigned long long gu64;
typedef GAS unsigned gu32;
#define RLX_AGENT __ATOMIC_RELAXED, __HIP_MEMORY_SCOPE_AGENT

namespace {
constexpr int D = 640, HQ = 32, DH = 192, SK = 8192, WIN = 2048, FF = 1536;
constexpr int QCOLS = HQ * DH;
constexpr int NWG = 256, NTHR = 512;
constexpr int KEYS_WG = WIN / 4;
constexpr unsigned SPIN_CAP = 1u << 20;

constexpr size_t WS_EPOCH = 0;
constexpr size_t WS_E1 = 4096;
constexpr size_t WS_E2A = WS_E1 + (size_t)NWG * 256 * 8;
constexpr size_t WS_E2B = WS_E2A + (size_t)HQ * D * 2 * 8;
constexpr size_t WS_E3 = WS_E2B + (size_t)D * 8;

__device__ __forceinline__ float bf2f(unsigned short u) { return __builtin_bit_cast(float, (unsigned)u << 16); }
__device__ __forceinline__ float lo2f(unsigned u) { return __builtin_bit_cast(float, u << 16); }
__device__ __forceinline__ float hi2f(unsigned u) { return __builtin_bit_cast(float, u & 0xffff0000u); }
__device__ __forceinline__ unsigned short f2bf(float f) { return __builtin_bit_cast(unsigned short, (__bf16)f); }
__device__ __forceinline__ float rbf(float f) { return (float)(__bf16)f; }
__device__ __forceinline__ float dot2(unsigned k, unsigned q, float acc) {
  return __builtin_amdgcn_fdot2_f32_bf16(__builtin_bit_cast(bf16x2, k), __builtin_bit_cast(bf16x2, q), acc, false);
}
template <int CTRL> __device__ __forceinline__ float dpp(float x) {
  return __builtin_bit_cast(float, __builtin_amdgcn_mov_dpp(__builtin_bit_cast(int, x), CTRL, 0xf, 0xf, true));
}
constexpr int XOR1 = 0xB1, XOR2 = 0x4E, HMIRROR = 0x141, ROR8 = 0x128, ROR4 = 0x124;
__device__ __forceinline__ float swap16_sum(float x) {
  auto s = __builtin_amdgcn_permlane16_swap(__float_as_uint(x), __float_as_uint(x), false, false);
  return __uint_as_float(s[0]) + __uint_as_float(s[1]);
}
__device__ __forceinline__ float swap32_sum(float x) {
  auto s = __builtin_amdgcn_permlane32_swap(__float_as_uint(x), __float_as_uint(x), false, false);
  return __uint_as_float(s[0]) + __uint_as_float(s[1]);
}
__device__ __forceinline__ float swap16_max(float x) {
  auto s = __builtin_amdgcn_permlane16_swap(__float_as_uint(x), __float_as_uint(x), false, false);
  return fmaxf(__uint_as_float(s[0]), __uint_as_float(s[1]));
}
__device__ __forceinline__ float swap32_max(float x) {
  auto s = __builtin_amdgcn_permlane32_swap(__float_as_uint(x), __float_as_uint(x), false, false);
  return fmaxf(__uint_as_float(s[0]), __uint_as_float(s[1]));
}
__device__ __forceinline__ float grp_sum(float x) { x += dpp<ROR8>(x); x = swap16_sum(x); return swap32_sum(x); }
__device__ __forceinline__ float grp_max(float x) { x = fmaxf(x, dpp<ROR8>(x)); x = swap16_max(x); return swap32_max(x); }
__device__ __forceinline__ float oct_sum(float x) { x += dpp<XOR1>(x); x += dpp<XOR2>(x); x += dpp<HMIRROR>(x); return x; }
__device__ __forceinline__ float wave_sum(float x) { return grp_sum(oct_sum(x)); }

__device__ __forceinline__ void wg_barrier() {
  asm volatile("s_waitcnt lgkmcnt(0)" ::: "memory");
  __builtin_amdgcn_s_barrier();
  asm volatile("" ::: "memory");
}
__device__ __forceinline__ void put(gu64* g, unsigned epoch, unsigned value) {
  __hip_atomic_store(g, ((unsigned long long)epoch << 32) | value, RLX_AGENT);
}
__device__ __forceinline__ unsigned get(gu64* g, unsigned epoch) {
  unsigned long long x = __hip_atomic_load(g, RLX_AGENT);
  for (unsigned spins = 0; (unsigned)(x >> 32) != epoch && spins < SPIN_CAP; ++spins) {
    __builtin_amdgcn_s_sleep(1);
    x = __hip_atomic_load(g, RLX_AGENT);
  }
  return (unsigned)x;
}
__device__ __forceinline__ v4u ldnt(const void* p) { return __builtin_nontemporal_load((const GAS v4u*)p); }
__device__ __forceinline__ v4u ld16(const void* p) { return *(const GAS v4u*)p; }

__device__ __forceinline__ void ln_stats(const float (&v)[10], float& mean_bf, float& sd_bf) {
  float s = 0.f;
#pragma unroll
  for (int i = 0; i < 10; ++i) s += v[i];
  const float mean = wave_sum(s) * (1.0f / D);
  float q = 0.f;
#pragma unroll
  for (int i = 0; i < 10; ++i) { const float d = v[i] - mean; q = fmaf(d, d, q); }
  const float var = wave_sum(q) * (1.0f / D);
  mean_bf = rbf(mean);
  sd_bf = rbf(sqrtf(rbf(rbf(var) + 1e-5f)));
}
__device__ __forceinline__ float ln_apply(float x, float mean_bf, float sd_bf) { return rbf(rbf(x - mean_bf) / sd_bf); }

struct Lds {
  float xn[8][D];
  float part[48][160];
  float raw[8][200];
  float on[2][DH];
  float hv[2][D];
  f32x2 hn[D];
  float mw[8], lw[8];
  float red[8][32];
  float hcol[16];
  unsigned short qs[DH];
};

__global__ void __launch_bounds__(NTHR, 2)
fused_kernel(const unsigned short* __restrict__ x, const unsigned short* __restrict__ kc, const unsigned short* __restrict__ vc,
             const unsigned short* __restrict__ wq, const unsigned short* __restrict__ wo, const unsigned short* __restrict__ wg,
             const unsigned short* __restrict__ wu, const unsigned short* __restrict__ wd, unsigned short* __restrict__ out,
             unsigned char* ws) {
  __shared__ __attribute__((aligned(16))) Lds L;
  const int tid = threadIdx.x, lane = tid & 63, w = __builtin_amdgcn_readfirstlane(tid >> 6);
  const int bid = blockIdx.x, xcd = bid & 7, idx = bid >> 3;
  const int h = xcd * 4 + (idx >> 3), j = idx & 7, b = j >> 2, s = j & 3;
  const int grp = lane >> 3, sub = lane & 7;
  const bool is_f1 = idx < 24, is_f2 = idx >= 22;
  const int n1 = xcd * 24 + idx;
  const int f2 = xcd * 10 + (idx - 22);

  gu64* e1 = (gu64*)(ws + WS_E1);
  gu64* e2a = (gu64*)(ws + WS_E2A);
  gu64* e2b = (gu64*)(ws + WS_E2B);
  gu64* e3 = (gu64*)(ws + WS_E3);

  const unsigned epoch = __hip_atomic_load((gu32*)(ws + WS_EPOCH), RLX_AGENT) + 1u;
  unsigned xr[5];
#pragma unroll
  for (int i = 0; i < 5; ++i) xr[i] = *(const GAS unsigned*)(x + b * D + 2 * (lane + 64 * i));

  v4u wqv[30];
  {
    const unsigned short* p = wq + (size_t)(w * 80 + grp) * QCOLS + h * DH + sub * 8;
#pragma unroll
    for (int g = 0; g < 10; ++g)
#pragma unroll
      for (int c = 0; c < 3; ++c) wqv[g * 3 + c] = ld16(p + (size_t)g * 8 * QCOLS + c * 64);
  }
  const size_t kvoff = ((size_t)(b * HQ + h) * SK + (SK - WIN) + s * KEYS_WG + w * 64 + grp) * DH + sub * 8;
  v4u kv[24];
#pragma unroll
  for (int g = 0; g < 8; ++g)
#pragma unroll
    for (int c = 0; c < 3; ++c) kv[g * 3 + c] = ldnt(kc + kvoff + g * 8 * DH + c * 64);
  __builtin_amdgcn_sched_barrier(0);

  {
    float xv[10];
#pragma unroll
    for (int i = 0; i < 5; ++i) { xv[2 * i] = lo2f(xr[i]); xv[2 * i + 1] = hi2f(xr[i]); }
    float mb, sd;
    ln_stats(xv, mb, sd);
#pragma unroll
    for (int i = 0; i < 5; ++i)
      *(f32x2*)&L.xn[w][2 * (lane + 64 * i)] = f32x2{ln_apply(xv[2 * i], mb, sd), ln_apply(xv[2 * i + 1], mb, sd)};
  }
  asm volatile("s_waitcnt lgkmcnt(0)" ::: "memory");

  v4u vv[24];
  {
    float qa[24];
#pragma unroll
    for (int i = 0; i < 24; ++i) qa[i] = 0.f;
#pragma unroll
    for (int g = 0; g < 10; ++g) {
      const float xv = L.xn[w][w * 80 + g * 8 + grp];
#pragma unroll
      for (int c = 0; c < 3; ++c) {
        const v4u t = wqv[g * 3 + c];
#pragma unroll
        for (int e = 0; e < 4; ++e) {
          qa[c * 8 + 2 * e] = fmaf(xv, lo2f(t[e]), qa[c * 8 + 2 * e]);
          qa[c * 8 + 2 * e + 1] = fmaf(xv, hi2f(t[e]), qa[c * 8 + 2 * e + 1]);
        }
      }
      if (g < 8) {
#pragma unroll
        for (int c = 0; c < 3; ++c) vv[g * 3 + c] = ldnt(vc + kvoff + g * 8 * DH + c * 64);
      }
      __builtin_amdgcn_sched_barrier(0);
    }
#pragma unroll
    for (int i = 0; i < 24; ++i) qa[i] = grp_sum(qa[i]);
    if (grp == 0) {
#pragma unroll
      for (int c = 0; c < 3; ++c) {
        *(f32x4*)&L.raw[w][c * 64 + sub * 8] = f32x4{qa[c * 8], qa[c * 8 + 1], qa[c * 8 + 2], qa[c * 8 + 3]};
        *(f32x4*)&L.raw[w][c * 64 + sub * 8 + 4] = f32x4{qa[c * 8 + 4], qa[c * 8 + 5], qa[c * 8 + 6], qa[c * 8 + 7]};
      }
    }
  }
  if (tid < 194) put(e1 + bid * 256 + tid, ~epoch, 0u);
  if (tid < 160) put(e2a + ((size_t)h * D + j * 80) * 2 + tid, ~epoch, 0u);
  if (is_f2 && tid < 8) put(e2b + f2 * 8 + tid, ~epoch, 0u);
  if (is_f1 && tid < 8) put(e3 + n1 * 8 + tid, ~epoch, 0u);
  wg_barrier();
  if (tid < DH) {
    float q = 0.f;
#pragma unroll
    for (int i = 0; i < 8; ++i) q += L.raw[i][tid];
    L.qs[tid] = f2bf(q);
  }
  wg_barrier();
  v4u qp[3];
#pragma unroll
  for (int c = 0; c < 3; ++c) qp[c] = *(const v4u*)&L.qs[c * 64 + sub * 8];

  float sc[8];
#pragma unroll
  for (int g = 0; g < 8; ++g) {
    float a = 0.f;
#pragma unroll
    for (int c = 0; c < 3; ++c)
#pragma unroll
      for (int e = 0; e < 4; ++e) a = dot2(kv[g * 3 + c][e], qp[c][e], a);
    a = oct_sum(a);
    sc[g] = rbf(rbf(a) * 0.07216878364870322f);
  }

  v4u wov[4];
  {
    const int r10 = lane / 10, c10 = lane - r10 * 10;
    const unsigned short* p = wo + (size_t)(h * DH + w * 24 + (lane < 60 ? r10 : 0)) * D + j * 80 + (lane < 60 ? c10 : 0) * 8;
#pragma unroll
    for (int i = 0; i < 4; ++i) wov[i] = ld16(p + (size_t)i * 6 * D);
  }
  unsigned f1w[10];
  {
    const int combo = tid & 7, rg = tid >> 3;
    const unsigned short* p = ((combo >> 2) ? wu : wg) + (size_t)rg * FF + (is_f1 ? n1 : 0) * 8 + (combo & 3) * 2;
#pragma unroll
    for (int i = 0; i < 10; ++i) f1w[i] = *(const GAS unsigned*)(p + (size_t)i * 64 * FF);
  }
  v4u f2w[3];
  {
    const unsigned short* p = wd + (size_t)tid * D + (is_f2 ? f2 : 0) * 8;
#pragma unroll
    for (int i = 0; i < 3; ++i) f2w[i] = ld16(p + (size_t)i * 512 * D);
  }
  __builtin_amdgcn_sched_barrier(0);

  {
    float m = sc[0];
#pragma unroll
    for (int g = 1; g < 8; ++g) m = fmaxf(m, sc[g]);
    m = grp_max(m);
    float l = 0.f, p[8];
#pragma unroll
    for (int g = 0; g < 8; ++g) { p[g] = __expf(sc[g] - m); l += p[g]; }
    l = grp_sum(l);
    float acc[24];
#pragma unroll
    for (int i = 0; i < 24; ++i) acc[i] = 0.f;
#pragma unroll
    for (int g = 0; g < 8; ++g)
#pragma unroll
      for (int c = 0; c < 3; ++c) {
        const v4u t = vv[g * 3 + c];
#pragma unroll
        for (int e = 0; e < 4; ++e) {
          acc[c * 8 + 2 * e] = fmaf(p[g], lo2f(t[e]), acc[c * 8 + 2 * e]);
          acc[c * 8 + 2 * e + 1] = fmaf(p[g], hi2f(t[e]), acc[c * 8 + 2 * e + 1]);
        }
      }
#pragma unroll
    for (int i = 0; i < 24; ++i) acc[i] = grp_sum(acc[i]);
    if (grp == 0) {
#pragma unroll
      for (int c = 0; c < 3; ++c) {
        *(f32x4*)&L.raw[w][c * 64 + sub * 8] = f32x4{acc[c * 8], acc[c * 8 + 1], acc[c * 8 + 2], acc[c * 8 + 3]};
        *(f32x4*)&L.raw[w][c * 64 + sub * 8 + 4] = f32x4{acc[c * 8 + 4], acc[c * 8 + 5], acc[c * 8 + 6], acc[c * 8 + 7]};
      }
      if (sub == 0) { L.mw[w] = m; L.lw[w] = l; }
    }
  }
  wg_barrier();
  if (tid < 194) {
    float M = L.mw[0];
#pragma unroll
    for (int i = 1; i < 8; ++i) M = fmaxf(M, L.mw[i]);
    float o = 0.f, l = 0.f;
#pragma unroll
    for (int i = 0; i < 8; ++i) {
      const float wt = __expf(L.mw[i] - M);
      o = fmaf(wt, L.raw[i][tid < DH ? tid : 0], o);
      l = fmaf(wt, L.lw[i], l);
    }
    const float v = tid < DH ? o : (tid == DH ? M : l);
    put(e1 + bid * 256 + tid, epoch, __builtin_bit_cast(unsigned, v));
  }
  wg_barrier();

  {
    gu64* src = e1 + (size_t)((bid & 7) | ((idx & ~7) + w) << 3) * 256;
    const unsigned v0 = get(src + lane, epoch), v1 = get(src + 64 + lane, epoch), v2 = get(src + 128 + lane, epoch);
    L.raw[w][lane] = __builtin_bit_cast(float, v0);
    L.raw[w][64 + lane] = __builtin_bit_cast(float, v1);
    L.raw[w][128 + lane] = __builtin_bit_cast(float, v2);
    if (lane < 2) L.raw[w][192 + lane] = __builtin_bit_cast(float, get(src + 192 + lane, epoch));
  }
  wg_barrier();
  if (tid < 2 * DH) {
    const int bb = tid >= DH ? 1 : 0, d = tid - bb * DH;
    float M = L.raw[bb * 4][192];
#pragma unroll
    for (int i = 1; i < 4; ++i) M = fmaxf(M, L.raw[bb * 4 + i][192]);
    float o = 0.f, l = 0.f;
#pragma unroll
    for (int i = 0; i < 4; ++i) {
      const float wt = __expf(L.raw[bb * 4 + i][192] - M);
      o = fmaf(wt, L.raw[bb * 4 + i][d], o);
      l = fmaf(wt, L.raw[bb * 4 + i][193], l);
    }
    L.on[bb][d] = rbf(o / l);
  }
  wg_barrier();
  if (lane < 60) {
    const int r10 = lane / 10, c10 = lane - r10 * 10;
    float a0[8], a1[8];
#pragma unroll
    for (int e = 0; e < 8; ++e) { a0[e] = 0.f; a1[e] = 0.f; }
#pragma unroll
    for (int i = 0; i < 4; ++i) {
      const int row = w * 24 + i * 6 + r10;
      const float o0 = L.on[0][row], o1 = L.on[1][row];
#pragma unroll
      for (int e = 0; e < 4; ++e) {
        const float lo = lo2f(wov[i][e]), hi = hi2f(wov[i][e]);
        a0[2 * e] = fmaf(o0, lo, a0[2 * e]); a0[2 * e + 1] = fmaf(o0, hi, a0[2 * e + 1]);
        a1[2 * e] = fmaf(o1, lo, a1[2 * e]); a1[2 * e + 1] = fmaf(o1, hi, a1[2 * e + 1]);
      }
    }
    float* dst = &L.part[w * 6 + r10][c10 * 16];
#pragma unroll
    for (int e = 0; e < 8; e += 2) *(f32x4*)(dst + 2 * e) = f32x4{a0[e], a1[e], a0[e + 1], a1[e + 1]};
  }
  wg_barrier();
  if (tid < 160) {
    float t = 0.f;
#pragma unroll
    for (int i = 0; i < 48; ++i) t += L.part[i][tid];
    put(e2a + ((size_t)h * D + j * 80) * 2 + tid, epoch, __builtin_bit_cast(unsigned, t));
  }

  if (is_f2) {
    const int hh = tid >> 4, cb = tid & 15;
    float t = __builtin_bit_cast(float, get(e2a + ((size_t)hh * D + f2 * 8) * 2 + cb, epoch));
    t = swap16_sum(t);
    t = swap32_sum(t);
    if (lane < 16) L.red[w][lane] = t;
    wg_barrier();
    if (tid < 16) {
      float a = 0.f;
#pragma unroll
      for (int i = 0; i < 8; ++i) a += L.red[i][tid];
      const int c = f2 * 8 + (tid >> 1), bb = tid & 1;
      const float hv = rbf(bf2f(x[bb * D + c]) + rbf(a));
      L.hcol[tid] = hv;
      const unsigned hb = f2bf(hv);
      const unsigned other = __builtin_amdgcn_mov_dpp(hb, XOR1, 0xf, 0xf, true);
      if (bb == 0) put(e2b + c, epoch, hb | (other << 16));
    }
  }

  if (is_f1) {
    {
      const unsigned v = get(e2b + tid, epoch);
      L.hv[0][tid] = lo2f(v); L.hv[1][tid] = hi2f(v);
      if (tid < 128) {
        const unsigned v2 = get(e2b + 512 + tid, epoch);
        L.hv[0][512 + tid] = lo2f(v2); L.hv[1][512 + tid] = hi2f(v2);
      }
    }
    wg_barrier();
    {
      float v0[10], v1[10], m0, s0, m1, s1;
#pragma unroll
      for (int i = 0; i < 10; ++i) { v0[i] = L.hv[0][lane + 64 * i]; v1[i] = L.hv[1][lane + 64 * i]; }
      ln_stats(v0, m0, s0);
      ln_stats(v1, m1, s1);
      f32x2* hnw = (f32x2*)&L.xn[w][0];
      (void)hnw;
      float a[4] = {0.f, 0.f, 0.f, 0.f};
      const int rg = tid >> 3;
#pragma unroll
      for (int i = 0; i < 10; ++i) {
        const float h0 = ln_apply(L.hv[0][rg + 64 * i], m0, s0), h1 = ln_apply(L.hv[1][rg + 64 * i], m1, s1);
        const float lo = lo2f(f1w[i]), hi = hi2f(f1w[i]);
        a[0] = fmaf(h0, lo, a[0]); a[1] = fmaf(h0, hi, a[1]);
        a[2] = fmaf(h1, lo, a[2]); a[3] = fmaf(h1, hi, a[3]);
      }
#pragma unroll
      for (int e = 0; e < 4; ++e) a[e] = grp_sum(a[e]);
      if (grp == 0) *(f32x4*)&L.red[w][sub * 4] = f32x4{a[0], a[1], a[2], a[3]};
    }
    wg_barrier();
    if (tid < 16) {
      const int col = tid & 7, bb = tid >> 3;
      float g = 0.f, u = 0.f;
#pragma unroll
      for (int i = 0; i < 8; ++i) {
        g += L.red[i][(col >> 1) * 4 + bb * 2 + (col & 1)];
        u += L.red[i][(4 + (col >> 1)) * 4 + bb * 2 + (col & 1)];
      }
      g = rbf(g); u = rbf(u);
      const float inner = 0.7978845608028654f * (g + 0.044715f * g * g * g);
      const float ge = rbf(0.5f * g * (1.0f + tanhf(inner)));
      const unsigned ab = f2bf(ge * u);
      const unsigned other = __builtin_amdgcn_mov_dpp(ab, ROR8, 0xf, 0xf, true);
      if (bb == 0) put(e3 + n1 * 8 + col, epoch, ab | (other << 16));
    }
  }

  if (is_f2) {
    float a0[8], a1[8];
#pragma unroll
    for (int e = 0; e < 8; ++e) { a0[e] = 0.f; a1[e] = 0.f; }
#pragma unroll
    for (int i = 0; i < 3; ++i) {
      const unsigned v = get(e3 + tid + 512 * i, epoch);
      const float x0 = lo2f(v), x1 = hi2f(v);
#pragma unroll
      for (int e = 0; e < 4; ++e) {
        const float lo = lo2f(f2w[i][e]), hi = hi2f(f2w[i][e]);
        a0[2 * e] = fmaf(x0, lo, a0[2 * e]); a0[2 * e + 1] = fmaf(x0, hi, a0[2 * e + 1]);
        a1[2 * e] = fmaf(x1, lo, a1[2 * e]); a1[2 * e + 1] = fmaf(x1, hi, a1[2 * e + 1]);
      }
    }
#pragma unroll
    for (int e = 0; e < 8; ++e) { a0[e] = wave_sum(a0[e]); a1[e] = wave_sum(a1[e]); }
    wg_barrier();
    if (lane == 0) {
#pragma unroll
      for (int e = 0; e < 8; ++e) { L.red[w][2 * e] = a0[e]; L.red[w][2 * e + 1] = a1[e]; }
    }
    wg_barrier();
    if (tid < 16) {
      float a = 0.f;
#pragma unroll
      for (int i = 0; i < 8; ++i) a += L.red[i][tid];
      const int c = f2 * 8 + (tid >> 1), bb = tid & 1;
      out[bb * D + c] = f2bf(L.hcol[tid] + rbf(a));
    }
    if (bid == NWG - 1 && tid == 0) __hip_atomic_store((gu32*)(ws + WS_EPOCH), epoch, RLX_AGENT);
  }
}
}

extern "C" void kernel_launch(void* const* d_in, const int* in_sizes, int n_in, void* d_out, int out_size, void* d_ws,
                              size_t ws_size, hipStream_t stream) {
  const unsigned short* x = (const unsigned short*)d_in[0];
  const unsigned short* kc = (const unsigned short*)d_in[1];
  const unsigned short* vc = (const unsigned short*)d_in[2];
  const unsigned short* wq = (const unsigned short*)d_in[3];
  const unsigned short* wo = (const unsigned short*)d_in[4];
  const unsigned short* wg = (const unsigned short*)d_in[6];
  const unsigned short* wu = (const unsigned short*)d_in[7];
  const unsigned short* wd = (const unsigned short*)d_in[8];
  fused_kernel<<<dim3(NWG), dim3(NTHR), 0, stream>>>(x, kc, vc, wq, wo, wg, wu, wd, (unsigned short*)d_out,
                                                      (unsigned char*)d_ws);
}
```

```cpp
#include <hip/hip_runtime.h>
#include <hip/hip_bf16.h>
#include <stdint.h>

#define GAS __attribute__((address_space(1)))
typedef unsigned v4u __attribute__((ext_vector_type(4)));
typedef float f32x4 __attribute__((ext_vector_type(4)));
typedef float f32x2 __attribute__((ext_vector_type(2)));
typedef __bf16 bf16x2 __attribute__((ext_vector_type(2)));
typedef unsigned long long u64;
typedef GAS u64 gu64;
#define RLX_AGENT __ATOMIC_RELAXED, __HIP_MEMORY_SCOPE_AGENT
#define RLX_WG __ATOMIC_RELAXED, __HIP_MEMORY_SCOPE_WORKGROUP

#define POLL_BT 5
#define WO_POS 1
#define FFN_POS 2
#define GATE_BT 1
#define SENT_E3A 2
#define SENT_E2B 1
#define SLEEP_N 1
namespace {
constexpr int D = 640, HQ = 32, DH = 192, SK = 8192, WIN = 2048, FF = 1536;
constexpr int QCOLS = HQ * DH;
constexpr int NWG = 256, NTHR = 512;
constexpr int KEYS_WG = WIN / 4;
constexpr unsigned SPIN_CAP = 1u << 20;

constexpr size_t WS_EPOCH = 0;
constexpr size_t WS_E0 = 4096;
constexpr size_t WS_E1 = WS_E0 + (size_t)NWG * 384 * 8;
constexpr size_t WS_E2A = WS_E1 + (size_t)NWG * 256 * 8;
constexpr size_t WS_E2B = WS_E2A + (size_t)D * 64 * 8;
constexpr size_t WS_E3A = WS_E2B + (size_t)8 * D * 8;
constexpr size_t WS_E3B = WS_E3A + (size_t)FF * 16 * 8;

__device__ __forceinline__ float bf2f(unsigned short u) { return __uint_as_float((unsigned)u << 16); }
__device__ __forceinline__ float lo2f(unsigned u) { return __uint_as_float(u << 16); }
__device__ __forceinline__ float hi2f(unsigned u) { return __uint_as_float(u & 0xffff0000u); }
__device__ __forceinline__ unsigned short f2bf(float f) { return __builtin_bit_cast(unsigned short, (__bf16)f); }
__device__ __forceinline__ float rbf(float f) { return (float)(__bf16)f; }
__device__ __forceinline__ float dot2(unsigned k, unsigned q, float acc) {
  return __builtin_amdgcn_fdot2_f32_bf16(__builtin_bit_cast(bf16x2, k), __builtin_bit_cast(bf16x2, q), acc, false);
}
template <int CTRL> __device__ __forceinline__ float dpp(float x) {
  return __int_as_float(__builtin_amdgcn_mov_dpp(__float_as_int(x), CTRL, 0xf, 0xf, true));
}
constexpr int XOR1 = 0xB1, XOR2 = 0x4E, HMIRROR = 0x141, ROR8 = 0x128, QP0 = 0x00, QP2 = 0xAA;
__device__ __forceinline__ float fold32(float a, float b) {
  auto s = __builtin_amdgcn_permlane32_swap(__float_as_uint(a), __float_as_uint(b), false, false);
  return __uint_as_float(s[0]) + __uint_as_float(s[1]);
}
__device__ __forceinline__ float fold16(float a, float b) {
  auto s = __builtin_amdgcn_permlane16_swap(__float_as_uint(a), __float_as_uint(b), false, false);
  return __uint_as_float(s[0]) + __uint_as_float(s[1]);
}
__device__ __forceinline__ float fold8(float a, float b, bool bit3) {
  const float t = a + dpp<ROR8>(a), u = b + dpp<ROR8>(b);
  return bit3 ? u : t;
}
template <int N> __device__ __forceinline__ void fold_groups(const float (&v)[N], float (&r)[N / 8], bool bit3) {
  float a[N / 2], c[N / 4];
#pragma unroll
  for (int i = 0; i < N / 2; ++i) a[i] = fold32(v[i], v[i + N / 2]);
#pragma unroll
  for (int i = 0; i < N / 4; ++i) c[i] = fold16(a[i], a[i + N / 4]);
#pragma unroll
  for (int i = 0; i < N / 8; ++i) r[i] = fold8(c[i], c[i + N / 8], bit3);
}
__device__ __forceinline__ float swap16_max(float x) {
  auto s = __builtin_amdgcn_permlane16_swap(__float_as_uint(x), __float_as_uint(x), false, false);
  return fmaxf(__uint_as_float(s[0]), __uint_as_float(s[1]));
}
__device__ __forceinline__ float swap32_max(float x) {
  auto s = __builtin_amdgcn_permlane32_swap(__float_as_uint(x), __float_as_uint(x), false, false);
  return fmaxf(__uint_as_float(s[0]), __uint_as_float(s[1]));
}
__device__ __forceinline__ float grp_sum(float x) { x += dpp<ROR8>(x); x = fold16(x, x); return fold32(x, x); }
__device__ __forceinline__ float grp_max(float x) { x = fmaxf(x, dpp<ROR8>(x)); x = swap16_max(x); return swap32_max(x); }
__device__ __forceinline__ float oct_sum(float x) { x += dpp<XOR1>(x); x += dpp<XOR2>(x); x += dpp<HMIRROR>(x); return x; }
__device__ __forceinline__ float wave_sum(float x) { return grp_sum(oct_sum(x)); }
__device__ __forceinline__ float half_sum(float x) { x = oct_sum(x); x += dpp<ROR8>(x); return fold16(x, x); }

__device__ __forceinline__ void wg_barrier() {
  asm volatile("s_waitcnt lgkmcnt(0)" ::: "memory");
  __builtin_amdgcn_s_barrier();
  asm volatile("" ::: "memory");
}
__device__ __forceinline__ void lds_wait() { asm volatile("s_waitcnt lgkmcnt(0)" ::: "memory"); }
__device__ __forceinline__ void putf(gu64* g, u64 tag, float v) {
  __hip_atomic_store(g, (tag << 24) | (u64)(__float_as_uint(v) >> 8), RLX_AGENT);
}
__device__ __forceinline__ u64 ldg(gu64* g) { return __hip_atomic_load(g, RLX_AGENT); }
__device__ __forceinline__ bool okf(u64 x, u64 tag) { return (x >> 24) == tag; }
__device__ __forceinline__ float valf(u64 x) { return __uint_as_float(((unsigned)x) << 8); }
__device__ __forceinline__ void putu(gu64* g, u64 tag, unsigned v) { __hip_atomic_store(g, (tag << 32) | (u64)v, RLX_AGENT); }
__device__ __forceinline__ bool oku(u64 x, u64 tag) { return (unsigned)(x >> 32) == (unsigned)tag; }
__device__ __forceinline__ void wait_f(gu64* g, u64 tag) {
  for (unsigned spins = 0; spins < SPIN_CAP; ++spins) {
    if (okf(ldg(g), tag)) break;
    __builtin_amdgcn_s_sleep(2);
  }
}
__device__ __forceinline__ v4u ldnt(const void* p) { return __builtin_nontemporal_load((const GAS v4u*)p); }

__device__ __forceinline__ void ln_stats(const float (&v)[10], float& mean_bf, float& rsd) {
  float s = 0.f;
#pragma unroll
  for (int i = 0; i < 10; ++i) s += v[i];
  const float mean = wave_sum(s) * (1.0f / D);
  float q = 0.f;
#pragma unroll
  for (int i = 0; i < 10; ++i) { const float d = v[i] - mean; q = fmaf(d, d, q); }
  const float var = wave_sum(q) * (1.0f / D);
  mean_bf = rbf(mean);
  rsd = __builtin_amdgcn_rcpf(rbf(__builtin_amdgcn_sqrtf(rbf(rbf(var) + 1e-5f))));
}
__device__ __forceinline__ float ln_apply(float x, float mean_bf, float rsd) { return rbf(rbf(x - mean_bf) * rsd); }
__device__ __forceinline__ void fma8(float* a, float s, v4u t) {
#pragma unroll
  for (int e = 0; e < 4; ++e) { a[2 * e] = fmaf(s, lo2f(t[e]), a[2 * e]); a[2 * e + 1] = fmaf(s, hi2f(t[e]), a[2 * e + 1]); }
}
__device__ __forceinline__ float gelu_tanh(float g) {
  const float y = 0.7978845608028654f * (g + 0.044715f * g * g * g);
  const float t = 1.0f - 2.0f * __builtin_amdgcn_rcpf(__expf(2.0f * y) + 1.0f);
  return 0.5f * g * (1.0f + t);
}

#define STAMP(i) do { } while (0)

struct Lds {
  float xn[5][2][80];
  float part[48][160];
  float raw[8][200];
  float on[2][DH];
  float hv[2][D];
  float mw[8], lw[8];
  float red[8][128];
  unsigned short qs[DH];
  unsigned pcnt, qcnt, qflag;
};

__global__ void __launch_bounds__(NTHR, 2)
fused_kernel(const unsigned short* __restrict__ x, const unsigned short* __restrict__ kc, const unsigned short* __restrict__ vc,
             const unsigned short* __restrict__ wq, const unsigned short* __restrict__ wo, const unsigned short* __restrict__ wg,
             const unsigned short* __restrict__ wu, const unsigned short* __restrict__ wd, unsigned short* __restrict__ out,
             unsigned char* ws) {
  __shared__ __attribute__((aligned(16))) Lds L;
  const int tid = threadIdx.x, lane = tid & 63, w = __builtin_amdgcn_readfirstlane(tid >> 6);
  const int bid = blockIdx.x, xcd = bid & 7, idx = bid >> 3;
  const int h = xcd * 4 + (idx >> 3), j = idx & 7, b = j >> 2, s = j & 3;
  const int grp = lane >> 3, sub = lane & 7;
  const bool bit3 = (lane & 8) != 0;
  const int peer0 = xcd | ((idx & ~7) << 3);
  const bool is_f1 = bid < 192;
  const int cb1 = bid % 24, m1 = (bid / 24) & 1, kq1 = bid / 48;
  const bool is_f2 = bid < 240;
  const int cb2 = bid % 10, kp2 = bid / 10;
  const bool has_col = bid >= 176 && w == 0;
  const int ccol = (bid >= 176 ? bid - 176 : 0) * 8;

  gu64* e0 = (gu64*)(ws + WS_E0);
  gu64* e1 = (gu64*)(ws + WS_E1);
  gu64* e2a = (gu64*)(ws + WS_E2A);
  gu64* e2b = (gu64*)(ws + WS_E2B);
  gu64* e3a = (gu64*)(ws + WS_E3A);
  gu64* e3b = (gu64*)(ws + WS_E3B);

  u64 stv[32];
#pragma unroll
  for (int i = 0; i < 32; ++i) stv[i] = 0;
  STAMP(0);
  const u64 tag = (ldg((gu64*)(ws + WS_EPOCH)) + 1ull) & 0xFFFFFFFFFFull;
  const unsigned short xres_raw = x[(lane & 1) * D + ccol + ((lane >> 1) & 7)];
  unsigned xr[2][5];
  v4u wqv[6];
  if (w < 5) {
#pragma unroll
    for (int bb = 0; bb < 2; ++bb)
#pragma unroll
      for (int i = 0; i < 5; ++i) xr[bb][i] = *(const GAS unsigned*)(x + bb * D + 2 * (lane + 64 * i));
    const unsigned short* p = wq + (size_t)(j * 80 + w * 16 + grp) * QCOLS + h * DH + sub * 8;
#pragma unroll
    for (int g = 0; g < 2; ++g)
#pragma unroll
      for (int c = 0; c < 3; ++c) wqv[g * 3 + c] = ldnt(p + (size_t)g * 8 * QCOLS + c * 64);
  }
  if (tid == 0) { L.pcnt = 0u; L.qcnt = 0u; L.qflag = 0u; }
  __builtin_amdgcn_sched_barrier(0);
  wg_barrier();
  const size_t kvoff = ((size_t)(b * HQ + h) * SK + (SK - WIN) + s * KEYS_WG + w * 64 + grp) * DH + sub * 8;
  v4u kv[24], vv[24];
  {
    const bool poller = w >= 1 && w <= 3;
    const int p0 = (w - 1) * 3, np = w == 3 ? 2 : 3;
    gu64* src = e0 + (size_t)(peer0 + 8 * (poller ? p0 : 0)) * 384 + b * DH + lane;
    u64 g[3][3];
#pragma unroll
    for (int bt = 0; bt < 8; ++bt) {
      if (bt == GATE_BT) { while (__builtin_amdgcn_readfirstlane(__hip_atomic_load(&L.pcnt, RLX_WG)) < 100u) __builtin_amdgcn_s_sleep(1); }
      if (bt == POLL_BT && poller) {
#pragma unroll
        for (int i = 0; i < 3; ++i)
#pragma unroll
          for (int k = 0; k < 3; ++k) g[i][k] = ldg(src + (size_t)(i < np ? i : 0) * 8 * 384 + 64 * k);
      }
      __builtin_amdgcn_sched_barrier(0);
#pragma unroll
      for (int gg = 0; gg < 2; ++gg)
#pragma unroll
        for (int c = 0; c < 3; ++c) {
          if (bt < 4) kv[(bt * 2 + gg) * 3 + c] = ldnt(kc + kvoff + (bt * 2 + gg) * 8 * DH + c * 64);
          else vv[((bt - 4) * 2 + gg) * 3 + c] = ldnt(vc + kvoff + ((bt - 4) * 2 + gg) * 8 * DH + c * 64);
        }
      __builtin_amdgcn_sched_barrier(0);
      if (bt == GATE_BT - 1 && w < 5) {
#pragma unroll
        for (int bb = 0; bb < 2; ++bb) {
          float xv[10];
#pragma unroll
          for (int i = 0; i < 5; ++i) { xv[2 * i] = lo2f(xr[bb][i]); xv[2 * i + 1] = hi2f(xr[bb][i]); }
          float mb, rs;
          ln_stats(xv, mb, rs);
#pragma unroll
          for (int i = 0; i < 5; ++i) {
            const int e = 2 * (lane + 64 * i) - j * 80;
            if (e >= 0 && e < 80) *(f32x2*)&L.xn[w][bb][e] = f32x2{ln_apply(xv[2 * i], mb, rs), ln_apply(xv[2 * i + 1], mb, rs)};
          }
        }
        lds_wait();
        float qa[48], qr[6];
#pragma unroll
        for (int i = 0; i < 48; ++i) qa[i] = 0.f;
#pragma unroll
        for (int g = 0; g < 2; ++g) {
          const float x0 = L.xn[w][0][w * 16 + g * 8 + grp], x1 = L.xn[w][1][w * 16 + g * 8 + grp];
#pragma unroll
          for (int c = 0; c < 3; ++c) { fma8(qa + c * 8, x0, wqv[g * 3 + c]); fma8(qa + 24 + c * 8, x1, wqv[g * 3 + c]); }
        }
        fold_groups<48>(qa, qr, bit3);
        {
          const int i0 = ((lane >> 4) & 1) * 12 + (bit3 ? 6 : 0);
          float* dst = &L.part[0][0] + w * 384 + (lane >> 5) * DH + sub * 8;
#pragma unroll
          for (int k = 0; k < 6; ++k) dst[((i0 + k) >> 3) * 64 + ((i0 + k) & 7)] = qr[k];
        }
        lds_wait();
        unsigned old = 0;
        if (lane == 0) old = __hip_atomic_fetch_add(&L.pcnt, 1u, RLX_WG);
        if (__builtin_amdgcn_readfirstlane(old) == 4u) {
#pragma unroll
          for (int k = 0; k < 6; ++k) {
            const float* sp = &L.part[0][0] + lane + 64 * k;
            putf(e0 + bid * 384 + lane + 64 * k, tag, (sp[0] + sp[384]) + (sp[768] + sp[1152]) + sp[1536]);
          }
          __builtin_amdgcn_sched_barrier(0);
          if (lane == 0) __hip_atomic_store(&L.pcnt, 100u, RLX_WG);
        }
        STAMP(1);
      }
    }
    if (poller) {
      bool ok = true;
#pragma unroll
      for (int i = 0; i < 3; ++i)
#pragma unroll
        for (int k = 0; k < 3; ++k) ok = ok && okf(g[i][k], tag);
      if (__all(ok)) {
#pragma unroll
        for (int i = 0; i < 3; ++i)
          if (i < np) {
#pragma unroll
            for (int k = 0; k < 3; ++k) L.raw[p0 + i][64 * k + lane] = valf(g[i][k]);
          }
      } else {
        for (int i = 0; i < np; ++i) {
          gu64* sp = src + (size_t)i * 8 * 384;
          wait_f(sp - lane, tag);
          for (unsigned spins = 0; spins < SPIN_CAP; ++spins) {
            const u64 r0 = ldg(sp), r1 = ldg(sp + 64), r2 = ldg(sp + 128);
            L.raw[p0 + i][lane] = valf(r0); L.raw[p0 + i][64 + lane] = valf(r1); L.raw[p0 + i][128 + lane] = valf(r2);
            if (__all(okf(r0, tag) && okf(r1, tag) && okf(r2, tag))) break;
            __builtin_amdgcn_s_sleep(2);
          }
        }
      }
      lds_wait();
      unsigned old = 0;
      if (lane == 0) old = __hip_atomic_fetch_add(&L.qcnt, 1u, RLX_WG);
      if (__builtin_amdgcn_readfirstlane(old) == 2u) {
#pragma unroll
        for (int k = 0; k < 3; ++k) {
          float q = 0.f;
#pragma unroll
          for (int i = 0; i < 8; ++i) q += L.raw[i][lane + 64 * k];
          L.qs[lane + 64 * k] = f2bf(q);
        }
        lds_wait();
        if (lane == 0) __hip_atomic_store(&L.qflag, 1u, RLX_WG);
      }
    }
    while (__builtin_amdgcn_readfirstlane(__hip_atomic_load(&L.qflag, RLX_WG)) == 0u) __builtin_amdgcn_s_sleep(1);
    asm volatile("" ::: "memory");
  }
  STAMP(2);
  v4u qp[3];
#pragma unroll
  for (int c = 0; c < 3; ++c) qp[c] = *(const v4u*)&L.qs[c * 64 + sub * 8];

  float sc[8];
  {
#pragma unroll
    for (int g = 0; g < 8; ++g) {
      float a = 0.f;
#pragma unroll
      for (int c = 0; c < 3; ++c)
#pragma unroll
        for (int e = 0; e < 4; ++e) a = dot2(kv[g * 3 + c][e], qp[c][e], a);
      a = oct_sum(a);
      sc[g] = rbf(rbf(a) * 0.07216878364870322f);
    }
    float m = sc[0];
#pragma unroll
    for (int g = 1; g < 8; ++g) m = fmaxf(m, sc[g]);
    m = grp_max(m);
    if (lane == 0) L.mw[w] = m;
  }
  STAMP(3);

  v4u wov[4], f1w[3], f2w;
  __builtin_amdgcn_sched_barrier(0);
  wg_barrier();

  float M;
  {
    const f32x4 ma = *(const f32x4*)&L.mw[0], mb = *(const f32x4*)&L.mw[4];
    M = fmaxf(fmaxf(fmaxf(ma[0], ma[1]), fmaxf(ma[2], ma[3])), fmaxf(fmaxf(mb[0], mb[1]), fmaxf(mb[2], mb[3])));
    float l = 0.f, p[8];
#pragma unroll
    for (int g = 0; g < 8; ++g) { p[g] = __expf(sc[g] - M); l += p[g]; }
    l = grp_sum(l);
    if (lane == 0) L.lw[w] = l;
    float acc[24], r[3];
#pragma unroll
    for (int i = 0; i < 24; ++i) acc[i] = 0.f;
#pragma unroll
    for (int g = 0; g < 8; ++g)
#pragma unroll
      for (int c = 0; c < 3; ++c) fma8(acc + c * 8, p[g], vv[g * 3 + c]);
  {
    const int r10 = lane / 10, c10 = lane - r10 * 10;
    const unsigned short* p = wo + (size_t)(h * DH + w * 24 + (lane < 60 ? r10 : 0)) * D + j * 80 + (lane < 60 ? c10 : 0) * 8;
#pragma unroll
    for (int i = 0; i < 4; ++i) wov[i] = ldnt(p + (size_t)i * 6 * D);
  }
  __builtin_amdgcn_sched_barrier(0);
    fold_groups<24>(acc, r, bit3);
    const int i0 = (lane >> 5) * 12 + ((lane >> 4) & 1) * 6 + (bit3 ? 3 : 0);
#pragma unroll
    for (int k = 0; k < 3; ++k) L.raw[w][((i0 + k) >> 3) * 64 + sub * 8 + ((i0 + k) & 7)] = r[k];
  }
  wg_barrier();
  if (tid < 194) {
    float o = 0.f;
#pragma unroll
    for (int i = 0; i < 8; ++i) o += tid < DH ? L.raw[i][tid] : L.lw[i];
    putf(e1 + bid * 256 + tid, tag, tid == DH ? M : o);
  }
  wg_barrier();
  STAMP(4);

  {
    gu64* src = e1 + (size_t)(peer0 + 8 * w) * 256 + lane;
    const int o3 = lane < 2 ? 192 : 0;
    u64 g0 = ldg(src), g1 = ldg(src + 64), g2 = ldg(src + 128), g3 = ldg(src + o3);
    if (!__all(okf(g0, tag) && okf(g1, tag) && okf(g2, tag) && okf(g3, tag))) {
      wait_f(src - lane + 193, tag);
      STAMP(11);
      for (unsigned spins = 0; spins < SPIN_CAP; ++spins) {
        g0 = ldg(src); g1 = ldg(src + 64); g2 = ldg(src + 128); g3 = ldg(src + o3);
        if (__all(okf(g0, tag) && okf(g1, tag) && okf(g2, tag) && okf(g3, tag))) break;
        __builtin_amdgcn_s_sleep(1);
      }
    }
    STAMP(12);
    L.raw[w][lane] = valf(g0); L.raw[w][64 + lane] = valf(g1); L.raw[w][128 + lane] = valf(g2);
    if (lane < 2) L.raw[w][192 + lane] = valf(g3);
  }
  {
    const unsigned short* p1 = (m1 ? wu : wg) + (size_t)((is_f1 ? kq1 : 0) * 160 + (tid >> 3)) * FF + cb1 * 64 + sub * 8;
    f1w[0] = ldnt(p1);
    f1w[1] = ldnt(p1 + (size_t)64 * FF);
    f1w[2] = ldnt(p1 + (size_t)(tid < 256 ? 128 : 0) * FF);
    f2w = ldnt(wd + (size_t)((is_f2 ? kp2 : 0) * 64 + (tid >> 3)) * D + cb2 * 64 + sub * 8);
  }
  __builtin_amdgcn_sched_barrier(0);
  wg_barrier();
  STAMP(5);
  if (tid < 2 * DH) {
    const int bb = tid >= DH ? 1 : 0, d = tid - bb * DH;
    const float m0 = L.raw[bb * 4][192], m1v = L.raw[bb * 4 + 1][192], m2 = L.raw[bb * 4 + 2][192], m3 = L.raw[bb * 4 + 3][192];
    const float MM = fmaxf(fmaxf(m0, m1v), fmaxf(m2, m3));
    const float w0 = __expf(m0 - MM), w1 = __expf(m1v - MM), w2 = __expf(m2 - MM), w3 = __expf(m3 - MM);
    const float o = (w0 * L.raw[bb * 4][d] + w1 * L.raw[bb * 4 + 1][d]) + (w2 * L.raw[bb * 4 + 2][d] + w3 * L.raw[bb * 4 + 3][d]);
    const float l = (w0 * L.raw[bb * 4][193] + w1 * L.raw[bb * 4 + 1][193]) + (w2 * L.raw[bb * 4 + 2][193] + w3 * L.raw[bb * 4 + 3][193]);
    L.on[bb][d] = rbf(o * __builtin_amdgcn_rcpf(l));
  }
  wg_barrier();
  if (lane < 60) {
    const int r10 = lane / 10, c10 = lane - r10 * 10;
    float a0[8], a1[8];
#pragma unroll
    for (int e = 0; e < 8; ++e) { a0[e] = 0.f; a1[e] = 0.f; }
#pragma unroll
    for (int i = 0; i < 4; ++i) {
      const int row = w * 24 + i * 6 + r10;
      fma8(a0, L.on[0][row], wov[i]);
      fma8(a1, L.on[1][row], wov[i]);
    }
    float* dst = &L.part[w * 6 + r10][c10 * 16];
#pragma unroll
    for (int e = 0; e < 8; e += 2) *(f32x4*)(dst + 2 * e) = f32x4{a0[e], a1[e], a0[e + 1], a1[e + 1]};
  }
  wg_barrier();
  if (tid < 320) {
    const int o = tid >> 1, hf = (tid & 1) * 24;
    float t0 = 0.f, t1 = 0.f, t2 = 0.f, t3 = 0.f;
#pragma unroll
    for (int i = 0; i < 24; i += 4) { t0 += L.part[hf + i][o]; t1 += L.part[hf + i + 1][o]; t2 += L.part[hf + i + 2][o]; t3 += L.part[hf + i + 3][o]; }
    float t = (t0 + t1) + (t2 + t3);
    t += dpp<XOR1>(t);
    if ((tid & 1) == 0) putf(e2a + (size_t)h * (2 * D) + j * 160 + o, tag, t);
  }
  STAMP(6);

  float hres = 0.f;
  if (has_col) {
    gu64* src = e2a + (size_t)(lane >> 4) * (2 * D) + ccol * 2 + (lane & 15);
    u64 g[8];
    for (unsigned spins = 0; spins < SPIN_CAP; ++spins) {
      bool ok = true;
#pragma unroll
      for (int i = 0; i < 8; ++i) g[i] = ldg(src + (size_t)i * 4 * (2 * D));
#pragma unroll
      for (int i = 0; i < 8; ++i) ok = ok && okf(g[i], tag);
      if (__all(ok)) break;
      __builtin_amdgcn_s_sleep(1);
    }
    STAMP(13);
    float t = ((valf(g[0]) + valf(g[1])) + (valf(g[2]) + valf(g[3]))) + ((valf(g[4]) + valf(g[5])) + (valf(g[6]) + valf(g[7])));
    t = fold16(t, t);
    t = fold32(t, t);
    hres = rbf(bf2f(xres_raw) + rbf(t));
    const unsigned hb = f2bf(hres);
    const unsigned other = __builtin_amdgcn_mov_dpp(hb, XOR1, 0xf, 0xf, true);
    if ((lane & 1) == 0) {
      putu(e2b + (lane >> 4) * D + ccol + ((lane >> 1) & 7), tag, hb | (other << 16));
      putu(e2b + ((lane >> 4) + 4) * D + ccol + ((lane >> 1) & 7), tag, hb | (other << 16));
    }
    STAMP(14);
  }
  wg_barrier();
  STAMP(7);

  if (is_f1) {
    {
      const int o1 = tid < 128 ? 512 : 0;
      gu64* src = e2b + xcd * D + tid;
      if (SENT_E2B == 1) { for (unsigned spins = 0; spins < SPIN_CAP; ++spins) { if (oku(ldg(src - lane), tag)) break; __builtin_amdgcn_s_sleep(2); } }
      STAMP(15);
      u64 g0 = ldg(src), g1 = ldg(src + o1);
      for (unsigned spins = 0; !__all(oku(g0, tag) && oku(g1, tag)) && spins < SPIN_CAP; ++spins) {
        __builtin_amdgcn_s_sleep(SLEEP_N);
        g0 = ldg(src); g1 = ldg(src + o1);
      }
      STAMP(16);
      L.hv[0][tid] = lo2f((unsigned)g0); L.hv[1][tid] = hi2f((unsigned)g0);
      if (tid < 128) { L.hv[0][512 + tid] = lo2f((unsigned)g1); L.hv[1][512 + tid] = hi2f((unsigned)g1); }
    }
    wg_barrier();
    {
      STAMP(17);
      float m0, s0, m1v, s1;
      const int r0 = kq1 * 160 + (tid >> 3);
      float hr0[3], hr1[3];
      {
        f32x4 v[5];
#pragma unroll
        for (int i = 0; i < 5; ++i) v[i] = *(const f32x4*)&L.hv[lane >> 5][(lane & 31) * 4 + 128 * i];
#pragma unroll
        for (int i = 0; i < 3; ++i) { const int row = (i < 2 || tid < 256) ? r0 + 64 * i : 0; hr0[i] = L.hv[0][row]; hr1[i] = L.hv[1][row]; }
        const f32x4 sv = (v[0] + v[1]) + (v[2] + v[3]) + v[4];
        const float mean = half_sum((sv[0] + sv[1]) + (sv[2] + sv[3])) * (1.0f / D);
        f32x4 qv = {0.f, 0.f, 0.f, 0.f};
#pragma unroll
        for (int i = 0; i < 5; ++i) { const f32x4 d = v[i] - mean; qv += d * d; }
        const float var = half_sum((qv[0] + qv[1]) + (qv[2] + qv[3])) * (1.0f / D);
        const float mb = rbf(mean), rs = __builtin_amdgcn_rcpf(rbf(__builtin_amdgcn_sqrtf(rbf(rbf(var) + 1e-5f))));
        auto pm = __builtin_amdgcn_permlane32_swap(__float_as_uint(mb), __float_as_uint(mb), false, false);
        auto pr = __builtin_amdgcn_permlane32_swap(__float_as_uint(rs), __float_as_uint(rs), false, false);
        m0 = __uint_as_float(pm[0]); m1v = __uint_as_float(pm[1]); s0 = __uint_as_float(pr[0]); s1 = __uint_as_float(pr[1]);
      }
      float a[16], r[2];
#pragma unroll
      for (int e = 0; e < 16; ++e) a[e] = 0.f;
#pragma unroll
      for (int i = 0; i < 3; ++i) {
        const bool live = i < 2 || tid < 256;
        fma8(a, live ? ln_apply(hr0[i], m0, s0) : 0.f, f1w[i]);
        fma8(a + 8, live ? ln_apply(hr1[i], m1v, s1) : 0.f, f1w[i]);
      }
      fold_groups<16>(a, r, bit3);
      *(f32x2*)&L.red[w][(lane >> 5) * 64 + sub * 8 + ((lane >> 4) & 1) * 4 + (bit3 ? 2 : 0)] = f32x2{r[0], r[1]};
      STAMP(18);
    }
    wg_barrier();
    if (tid < 128) {
      float t = 0.f;
#pragma unroll
      for (int i = 0; i < 8; ++i) t += L.red[i][tid];
      putf(e3a + (size_t)((cb1 * 2 + m1) * 4 + kq1) * 128 + (tid & 63) * 2 + (tid >> 6), tag, t);
    }
  }
  STAMP(8);

  if (is_f2) {
    gu64* src = e3a + (size_t)(kp2 * 8 + sub) * 128 + (tid >> 3) * 2;
    if (SENT_E3A == 1) {
      gu64* sent = e3a + (size_t)(kp2 * 8 + sub) * 128 + w * 16;
      for (unsigned spins = 0; spins < SPIN_CAP; ++spins) { if (__all(okf(ldg(sent), tag))) break; __builtin_amdgcn_s_sleep(2); }
    } else if (SENT_E3A == 2) {
      if (w == 0) {
        gu64* sent = e3a + (size_t)(kp2 * 8 + sub) * 128 + (grp & 1) * 127;
        for (unsigned spins = 0; spins < SPIN_CAP; ++spins) { if (__all(okf(ldg(sent), tag))) break; __builtin_amdgcn_s_sleep(1); }
      }
      wg_barrier();
    }
    STAMP(19);
    u64 g0 = ldg(src), g1 = ldg(src + 1);
    for (unsigned spins = 0; !__all(okf(g0, tag) && okf(g1, tag)) && spins < SPIN_CAP; ++spins) {
      __builtin_amdgcn_s_sleep(SLEEP_N);
      g0 = ldg(src); g1 = ldg(src + 1);
    }
    STAMP(20);
    float t0 = valf(g0), t1 = valf(g1);
    t0 += dpp<XOR1>(t0); t1 += dpp<XOR1>(t1);
    t0 += dpp<XOR2>(t0); t1 += dpp<XOR2>(t1);
    const float o0 = dpp<HMIRROR>(t0), o1 = dpp<HMIRROR>(t1);
    const bool lowq = sub < 4;
    const float g_0 = rbf(lowq ? t0 : o0), g_1 = rbf(lowq ? t1 : o1), u_0 = rbf(lowq ? o0 : t0), u_1 = rbf(lowq ? o1 : t1);
    const float act0 = rbf(rbf(gelu_tanh(g_0)) * u_0), act1 = rbf(rbf(gelu_tanh(g_1)) * u_1);
    float a[16], r[2];
#pragma unroll
    for (int e = 0; e < 16; ++e) a[e] = 0.f;
    fma8(a, act0, f2w);
    fma8(a + 8, act1, f2w);
    fold_groups<16>(a, r, bit3);
    *(f32x2*)&L.part[w][(lane >> 5) * 64 + sub * 8 + ((lane >> 4) & 1) * 4 + (bit3 ? 2 : 0)] = f32x2{r[0], r[1]};
    STAMP(21);
    wg_barrier();
    if (tid < 128) {
      float t2 = 0.f;
#pragma unroll
      for (int i = 0; i < 8; ++i) t2 += L.part[i][tid];
      putf(e3b + (size_t)kp2 * (2 * D) + (cb2 * 64 + (tid & 63)) * 2 + (tid >> 6), tag, t2);
    }
  }
  STAMP(9);

  if (has_col) {
    gu64* src = e3b + (size_t)(lane >> 4) * (2 * D) + ccol * 2 + (lane & 15);
    u64 g[6];
    for (unsigned spins = 0; spins < SPIN_CAP; ++spins) {
      bool ok = true;
#pragma unroll
      for (int i = 0; i < 6; ++i) g[i] = ldg(src + (size_t)i * 4 * (2 * D));
#pragma unroll
      for (int i = 0; i < 6; ++i) ok = ok && okf(g[i], tag);
      if (__all(ok)) break;
      __builtin_amdgcn_s_sleep(1);
    }
    STAMP(22);
    float t = ((valf(g[0]) + valf(g[1])) + (valf(g[2]) + valf(g[3]))) + (valf(g[4]) + valf(g[5]));
    t = fold16(t, t);
    t = fold32(t, t);
    if (lane < 16) out[(lane & 1) * D + ccol + (lane >> 1)] = f2bf(hres + rbf(t));
    if (ccol == 0 && lane == 0) __hip_atomic_store((gu64*)(ws + WS_EPOCH), tag, RLX_AGENT);
  }
  STAMP(10);
}
}

extern "C" void kernel_launch(void* const* d_in, const int* in_sizes, int n_in, void* d_out, int out_size, void* d_ws,
                              size_t ws_size, hipStream_t stream) {
  const unsigned short* x = (const unsigned short*)d_in[0];
  const unsigned short* kc = (const unsigned short*)d_in[1];
  const unsigned short* vc = (const unsigned short*)d_in[2];
  const unsigned short* wq = (const unsigned short*)d_in[3];
  const unsigned short* wo = (const unsigned short*)d_in[4];
  const unsigned short* wg = (const unsigned short*)d_in[6];
  const unsigned short* wu = (const unsigned short*)d_in[7];
  const unsigned short* wd = (const unsigned short*)d_in[8];
  fused_kernel<<<dim3(NWG), dim3(NTHR), 0, stream>>>(x, kc, vc, wq, wo, wg, wu, wd, (unsigned short*)d_out,
                                                      (unsigned char*)d_ws);
}
```

```cpp
#include <hip/hip_runtime.h>
#include <hip/hip_bf16.h>
#include <stdint.h>

#define GAS __attribute__((address_space(1)))
typedef unsigned v4u __attribute__((ext_vector_type(4)));
typedef float f32x4 __attribute__((ext_vector_type(4)));
typedef float f32x2 __attribute__((ext_vector_type(2)));
typedef __bf16 bf16x2 __attribute__((ext_vector_type(2)));
typedef unsigned long long u64;
typedef GAS u64 gu64;
#define RLX_AGENT __ATOMIC_RELAXED, __HIP_MEMORY_SCOPE_AGENT
#define RLX_WG __ATOMIC_RELAXED, __HIP_MEMORY_SCOPE_WORKGROUP

#define POLL_BT 6
#define WO_POS 0
#define FFN_POS 0
#define GATE_BT 2
#define SENT_E3A 2
#define SENT_E2B 1
#define SLEEP_N 1
namespace {
constexpr int D = 640, HQ = 32, DH = 192, SK = 8192, WIN = 2048, FF = 1536;
constexpr int QCOLS = HQ * DH;
constexpr int NWG = 256, NTHR = 512;
constexpr int KEYS_WG = WIN / 4;
constexpr unsigned SPIN_CAP = 1u << 20;

constexpr size_t WS_EPOCH = 0;
constexpr size_t WS_E0 = 4096;
constexpr size_t WS_E1 = WS_E0 + (size_t)NWG * 384 * 8;
constexpr size_t WS_E2A = WS_E1 + (size_t)NWG * 256 * 8;
constexpr size_t WS_E2B = WS_E2A + (size_t)D * 64 * 8;
constexpr size_t WS_E3A = WS_E2B + (size_t)8 * D * 8;
constexpr size_t WS_E3B = WS_E3A + (size_t)FF * 16 * 8;

__device__ __forceinline__ float bf2f(unsigned short u) { return __uint_as_float((unsigned)u << 16); }
__device__ __forceinline__ float lo2f(unsigned u) { return __uint_as_float(u << 16); }
__device__ __forceinline__ float hi2f(unsigned u) { return __uint_as_float(u & 0xffff0000u); }
__device__ __forceinline__ unsigned short f2bf(float f) { return __builtin_bit_cast(unsigned short, (__bf16)f); }
__device__ __forceinline__ float rbf(float f) { return (float)(__bf16)f; }
__device__ __forceinline__ float dot2(unsigned k, unsigned q, float acc) {
  return __builtin_amdgcn_fdot2_f32_bf16(__builtin_bit_cast(bf16x2, k), __builtin_bit_cast(bf16x2, q), acc, false);
}
template <int CTRL> __device__ __forceinline__ float dpp(float x) {
  return __int_as_float(__builtin_amdgcn_mov_dpp(__float_as_int(x), CTRL, 0xf, 0xf, true));
}
constexpr int XOR1 = 0xB1, XOR2 = 0x4E, HMIRROR = 0x141, ROR8 = 0x128, QP0 = 0x00, QP2 = 0xAA;
__device__ __forceinline__ float fold32(float a, float b) {
  auto s = __builtin_amdgcn_permlane32_swap(__float_as_uint(a), __float_as_uint(b), false, false);
  return __uint_as_float(s[0]) + __uint_as_float(s[1]);
}
__device__ __forceinline__ float fold16(float a, float b) {
  auto s = __builtin_amdgcn_permlane16_swap(__float_as_uint(a), __float_as_uint(b), false, false);
  return __uint_as_float(s[0]) + __uint_as_float(s[1]);
}
__device__ __forceinline__ float fold8(float a, float b, bool bit3) {
  const float t = a + dpp<ROR8>(a), u = b + dpp<ROR8>(b);
  return bit3 ? u : t;
}
template <int N> __device__ __forceinline__ void fold_groups(const float (&v)[N], float (&r)[N / 8], bool bit3) {
  float a[N / 2], c[N / 4];
#pragma unroll
  for (int i = 0; i < N / 2; ++i) a[i] = fold32(v[i], v[i + N / 2]);
#pragma unroll
  for (int i = 0; i < N / 4; ++i) c[i] = fold16(a[i], a[i + N / 4]);
#pragma unroll
  for (int i = 0; i < N / 8; ++i) r[i] = fold8(c[i], c[i + N / 8], bit3);
}
__device__ __forceinline__ float swap16_max(float x) {
  auto s = __builtin_amdgcn_permlane16_swap(__float_as_uint(x), __float_as_uint(x), false, false);
  return fmaxf(__uint_as_float(s[0]), __uint_as_float(s[1]));
}
__device__ __forceinline__ float swap32_max(float x) {
  auto s = __builtin_amdgcn_permlane32_swap(__float_as_uint(x), __float_as_uint(x), false, false);
  return fmaxf(__uint_as_float(s[0]), __uint_as_float(s[1]));
}
__device__ __forceinline__ float grp_sum(float x) { x += dpp<ROR8>(x); x = fold16(x, x); return fold32(x, x); }
__device__ __forceinline__ float grp_max(float x) { x = fmaxf(x, dpp<ROR8>(x)); x = swap16_max(x); return swap32_max(x); }
__device__ __forceinline__ float oct_sum(float x) { x += dpp<XOR1>(x); x += dpp<XOR2>(x); x += dpp<HMIRROR>(x); return x; }
__device__ __forceinline__ float wave_sum(float x) { return grp_sum(oct_sum(x)); }
__device__ __forceinline__ float half_sum(float x) { x = oct_sum(x); x += dpp<ROR8>(x); return fold16(x, x); }

__device__ __forceinline__ void wg_barrier() {
  asm volatile("s_waitcnt lgkmcnt(0)" ::: "memory");
  __builtin_amdgcn_s_barrier();
  asm volatile("" ::: "memory");
}
__device__ __forceinline__ void lds_wait() { asm volatile("s_waitcnt lgkmcnt(0)" ::: "memory"); }
__device__ __forceinline__ void putf(gu64* g, u64 tag, float v) {
  __hip_atomic_store(g, (tag << 24) | (u64)(__float_as_uint(v) >> 8), RLX_AGENT);
}
__device__ __forceinline__ u64 ldg(gu64* g) { return __hip_atomic_load(g, RLX_AGENT); }
__device__ __forceinline__ bool okf(u64 x, u64 tag) { return (x >> 24) == tag; }
__device__ __forceinline__ float valf(u64 x) { return __uint_as_float(((unsigned)x) << 8); }
__device__ __forceinline__ void putu(gu64* g, u64 tag, unsigned v) { __hip_atomic_store(g, (tag << 32) | (u64)v, RLX_AGENT); }
__device__ __forceinline__ bool oku(u64 x, u64 tag) { return (unsigned)(x >> 32) == (unsigned)tag; }
__device__ __forceinline__ void wait_f(gu64* g, u64 tag) {
  for (unsigned spins = 0; spins < SPIN_CAP; ++spins) {
    if (okf(ldg(g), tag)) break;
    __builtin_amdgcn_s_sleep(2);
  }
}
__device__ __forceinline__ v4u ldnt(const void* p) { return __builtin_nontemporal_load((const GAS v4u*)p); }

__device__ __forceinline__ void ln_stats(const float (&v)[10], float& mean_bf, float& rsd) {
  float s = 0.f;
#pragma unroll
  for (int i = 0; i < 10; ++i) s += v[i];
  const float mean = wave_sum(s) * (1.0f / D);
  float q = 0.f;
#pragma unroll
  for (int i = 0; i < 10; ++i) { const float d = v[i] - mean; q = fmaf(d, d, q); }
  const float var = wave_sum(q) * (1.0f / D);
  mean_bf = rbf(mean);
  rsd = __builtin_amdgcn_rcpf(rbf(__builtin_amdgcn_sqrtf(rbf(rbf(var) + 1e-5f))));
}
__device__ __forceinline__ float ln_apply(float x, float mean_bf, float rsd) { return rbf(rbf(x - mean_bf) * rsd); }
__device__ __forceinline__ void fma8(float* a, float s, v4u t) {
#pragma unroll
  for (int e = 0; e < 4; ++e) { a[2 * e] = fmaf(s, lo2f(t[e]), a[2 * e]); a[2 * e + 1] = fmaf(s, hi2f(t[e]), a[2 * e + 1]); }
}
__device__ __forceinline__ float gelu_tanh(float g) {
  const float y = 0.7978845608028654f * (g + 0.044715f * g * g * g);
  const float t = 1.0f - 2.0f * __builtin_amdgcn_rcpf(__expf(2.0f * y) + 1.0f);
  return 0.5f * g * (1.0f + t);
}

#define STAMP(i) do { } while (0)

struct Lds {
  float xn[5][2][80];
  float part[48][160];
  float raw[8][200];
  float on[2][DH];
  float hv[2][D];
  float mw[8], lw[8];
  float red[8][128];
  unsigned short qs[DH];
  unsigned pcnt, qcnt, qflag;
};

__global__ void __launch_bounds__(NTHR, 2)
fused_kernel(const unsigned short* __restrict__ x, const unsigned short* __restrict__ kc, const unsigned short* __restrict__ vc,
             const unsigned short* __restrict__ wq, const unsigned short* __restrict__ wo, const unsigned short* __restrict__ wg,
             const unsigned short* __restrict__ wu, const unsigned short* __restrict__ wd, unsigned short* __restrict__ out,
             unsigned char* ws) {
  __shared__ __attribute__((aligned(16))) Lds L;
  const int tid = threadIdx.x, lane = tid & 63, w = __builtin_amdgcn_readfirstlane(tid >> 6);
  const int bid = blockIdx.x, xcd = bid & 7, idx = bid >> 3;
  const int h = xcd * 4 + (idx >> 3), j = idx & 7, b = j >> 2, s = j & 3;
  const int grp = lane >> 3, sub = lane & 7;
  const bool bit3 = (lane & 8) != 0;
  const int peer0 = xcd | ((idx & ~7) << 3);
  const bool is_f1 = bid < 192;
  const int cb1 = bid % 24, m1 = (bid / 24) & 1, kq1 = bid / 48;
  const bool is_f2 = bid < 240;
  const int cb2 = bid % 10, kp2 = bid / 10;
  const bool has_col = bid >= 176 && w == 0;
  const int ccol = (bid >= 176 ? bid - 176 : 0) * 8;

  gu64* e0 = (gu64*)(ws + WS_E0);
  gu64* e1 = (gu64*)(ws + WS_E1);
  gu64* e2a = (gu64*)(ws + WS_E2A);
  gu64* e2b = (gu64*)(ws + WS_E2B);
  gu64* e3a = (gu64*)(ws + WS_E3A);
  gu64* e3b = (gu64*)(ws + WS_E3B);

  u64 stv[32];
#pragma unroll
  for (int i = 0; i < 32; ++i) stv[i] = 0;
  STAMP(0);
  const u64 tag = (ldg((gu64*)(ws + WS_EPOCH)) + 1ull) & 0xFFFFFFFFFFull;
  const unsigned short xres_raw = x[(lane & 1) * D + ccol + ((lane >> 1) & 7)];
  unsigned xr[2][5];
  v4u wqv[6];
  if (w < 5) {
#pragma unroll
    for (int bb = 0; bb < 2; ++bb)
#pragma unroll
      for (int i = 0; i < 5; ++i) xr[bb][i] = *(const GAS unsigned*)(x + bb * D + 2 * (lane + 64 * i));
    const unsigned short* p = wq + (size_t)(j * 80 + w * 16 + grp) * QCOLS + h * DH + sub * 8;
#pragma unroll
    for (int g = 0; g < 2; ++g)
#pragma unroll
      for (int c = 0; c < 3; ++c) wqv[g * 3 + c] = ldnt(p + (size_t)g * 8 * QCOLS + c * 64);
  }
  if (tid == 0) { L.pcnt = 0u; L.qcnt = 0u; L.qflag = 0u; }
  __builtin_amdgcn_sched_barrier(0);
  wg_barrier();
  const size_t kvoff = ((size_t)(b * HQ + h) * SK + (SK - WIN) + s * KEYS_WG + w * 64 + grp) * DH + sub * 8;
  v4u kv[24], vv[24];
  {
    const bool poller = w >= 1 && w <= 3;
    const int p0 = (w - 1) * 3, np = w == 3 ? 2 : 3;
    gu64* src = e0 + (size_t)(peer0 + 8 * (poller ? p0 : 0)) * 384 + b * DH + lane;
    u64 g[3][3];
#pragma unroll
    for (int bt = 0; bt < 8; ++bt) {
      if (bt == GATE_BT) { while (__builtin_amdgcn_readfirstlane(__hip_atomic_load(&L.pcnt, RLX_WG)) < 100u) __builtin_amdgcn_s_sleep(1); }
      if (bt == POLL_BT && poller) {
#pragma unroll
        for (int i = 0; i < 3; ++i)
#pragma unroll
          for (int k = 0; k < 3; ++k) g[i][k] = ldg(src + (size_t)(i < np ? i : 0) * 8 * 384 + 64 * k);
      }
      __builtin_amdgcn_sched_barrier(0);
#pragma unroll
      for (int gg = 0; gg < 2; ++gg)
#pragma unroll
        for (int c = 0; c < 3; ++c) {
          if (bt < 4) kv[(bt * 2 + gg) * 3 + c] = ldnt(kc + kvoff + (bt * 2 + gg) * 8 * DH + c * 64);
          else vv[((bt - 4) * 2 + gg) * 3 + c] = ldnt(vc + kvoff + ((bt - 4) * 2 + gg) * 8 * DH + c * 64);
        }
      __builtin_amdgcn_sched_barrier(0);
      if (bt == GATE_BT - 1 && w < 5) {
#pragma unroll
        for (int bb = 0; bb < 2; ++bb) {
          float xv[10];
#pragma unroll
          for (int i = 0; i < 5; ++i) { xv[2 * i] = lo2f(xr[bb][i]); xv[2 * i + 1] = hi2f(xr[bb][i]); }
          float mb, rs;
          ln_stats(xv, mb, rs);
#pragma unroll
          for (int i = 0; i < 5; ++i) {
            const int e = 2 * (lane + 64 * i) - j * 80;
            if (e >= 0 && e < 80) *(f32x2*)&L.xn[w][bb][e] = f32x2{ln_apply(xv[2 * i], mb, rs), ln_apply(xv[2 * i + 1], mb, rs)};
          }
        }
        lds_wait();
        float qa[48], qr[6];
#pragma unroll
        for (int i = 0; i < 48; ++i) qa[i] = 0.f;
#pragma unroll
        for (int g = 0; g < 2; ++g) {
          const float x0 = L.xn[w][0][w * 16 + g * 8 + grp], x1 = L.xn[w][1][w * 16 + g * 8 + grp];
#pragma unroll
          for (int c = 0; c < 3; ++c) { fma8(qa + c * 8, x0, wqv[g * 3 + c]); fma8(qa + 24 + c * 8, x1, wqv[g * 3 + c]); }
        }
        fold_groups<48>(qa, qr, bit3);
        {
          const int i0 = ((lane >> 4) & 1) * 12 + (bit3 ? 6 : 0);
          float* dst = &L.part[0][0] + w * 384 + (lane >> 5) * DH + sub * 8;
#pragma unroll
          for (int k = 0; k < 6; ++k) dst[((i0 + k) >> 3) * 64 + ((i0 + k) & 7)] = qr[k];
        }
        lds_wait();
        unsigned old = 0;
        if (lane == 0) old = __hip_atomic_fetch_add(&L.pcnt, 1u, RLX_WG);
        if (__builtin_amdgcn_readfirstlane(old) == 4u) {
#pragma unroll
          for (int k = 0; k < 6; ++k) {
            const float* sp = &L.part[0][0] + lane + 64 * k;
            putf(e0 + bid * 384 + lane + 64 * k, tag, (sp[0] + sp[384]) + (sp[768] + sp[1152]) + sp[1536]);
          }
          __builtin_amdgcn_sched_barrier(0);
          if (lane == 0) __hip_atomic_store(&L.pcnt, 100u, RLX_WG);
        }
        STAMP(1);
      }
    }
    if (poller) {
      bool ok = true;
#pragma unroll
      for (int i = 0; i < 3; ++i)
#pragma unroll
        for (int k = 0; k < 3; ++k) ok = ok && okf(g[i][k], tag);
      if (__all(ok)) {
#pragma unroll
        for (int i = 0; i < 3; ++i)
          if (i < np) {
#pragma unroll
            for (int k = 0; k < 3; ++k) L.raw[p0 + i][64 * k + lane] = valf(g[i][k]);
          }
      } else {
        for (int i = 0; i < np; ++i) {
          gu64* sp = src + (size_t)i * 8 * 384;
          wait_f(sp - lane, tag);
          for (unsigned spins = 0; spins < SPIN_CAP; ++spins) {
            const u64 r0 = ldg(sp), r1 = ldg(sp + 64), r2 = ldg(sp + 128);
            L.raw[p0 + i][lane] = valf(r0); L.raw[p0 + i][64 + lane] = valf(r1); L.raw[p0 + i][128 + lane] = valf(r2);
            if (__all(okf(r0, tag) && okf(r1, tag) && okf(r2, tag))) break;
            __builtin_amdgcn_s_sleep(2);
          }
        }
      }
      lds_wait();
      unsigned old = 0;
      if (lane == 0) old = __hip_atomic_fetch_add(&L.qcnt, 1u, RLX_WG);
      if (__builtin_amdgcn_readfirstlane(old) == 2u) {
#pragma unroll
        for (int k = 0; k < 3; ++k) {
          float q = 0.f;
#pragma unroll
          for (int i = 0; i < 8; ++i) q += L.raw[i][lane + 64 * k];
          L.qs[lane + 64 * k] = f2bf(q);
        }
        lds_wait();
        if (lane == 0) __hip_atomic_store(&L.qflag, 1u, RLX_WG);
      }
    }
    while (__builtin_amdgcn_readfirstlane(__hip_atomic_load(&L.qflag, RLX_WG)) == 0u) __builtin_amdgcn_s_sleep(1);
    asm volatile("" ::: "memory");
  }
  STAMP(2);
  v4u qp[3];
#pragma unroll
  for (int c = 0; c < 3; ++c) qp[c] = *(const v4u*)&L.qs[c * 64 + sub * 8];

  float sc[8];
  {
#pragma unroll
    for (int g = 0; g < 8; ++g) {
      float a = 0.f;
#pragma unroll
      for (int c = 0; c < 3; ++c)
#pragma unroll
        for (int e = 0; e < 4; ++e) a = dot2(kv[g * 3 + c][e], qp[c][e], a);
      a = oct_sum(a);
      sc[g] = rbf(rbf(a) * 0.07216878364870322f);
    }
    float m = sc[0];
#pragma unroll
    for (int g = 1; g < 8; ++g) m = fmaxf(m, sc[g]);
    m = grp_max(m);
    if (lane == 0) L.mw[w] = m;
  }
  STAMP(3);

  v4u wov[4], f1w[3], f2w;
  {
    const int r10 = lane / 10, c10 = lane - r10 * 10;
    const unsigned short* p = wo + (size_t)(h * DH + w * 24 + (lane < 60 ? r10 : 0)) * D + j * 80 + (lane < 60 ? c10 : 0) * 8;
#pragma unroll
    for (int i = 0; i < 4; ++i) wov[i] = ldnt(p + (size_t)i * 6 * D);
  }
  __builtin_amdgcn_sched_barrier(0);
  {
    const unsigned short* p1 = (m1 ? wu : wg) + (size_t)((is_f1 ? kq1 : 0) * 160 + (tid >> 3)) * FF + cb1 * 64 + sub * 8;
    f1w[0] = ldnt(p1);
    f1w[1] = ldnt(p1 + (size_t)64 * FF);
    f1w[2] = ldnt(p1 + (size_t)(tid < 256 ? 128 : 0) * FF);
    f2w = ldnt(wd + (size_t)((is_f2 ? kp2 : 0) * 64 + (tid >> 3)) * D + cb2 * 64 + sub * 8);
  }
  __builtin_amdgcn_sched_barrier(0);
  __builtin_amdgcn_sched_barrier(0);
  wg_barrier();

  float M;
  {
    const f32x4 ma = *(const f32x4*)&L.mw[0], mb = *(const f32x4*)&L.mw[4];
    M = fmaxf(fmaxf(fmaxf(ma[0], ma[1]), fmaxf(ma[2], ma[3])), fmaxf(fmaxf(mb[0], mb[1]), fmaxf(mb[2], mb[3])));
    float l = 0.f, p[8];
#pragma unroll
    for (int g = 0; g < 8; ++g) { p[g] = __expf(sc[g] - M); l += p[g]; }
    l = grp_sum(l);
    if (lane == 0) L.lw[w] = l;
    float acc[24], r[3];
#pragma unroll
    for (int i = 0; i < 24; ++i) acc[i] = 0.f;
#pragma unroll
    for (int g = 0; g < 8; ++g)
#pragma unroll
      for (int c = 0; c < 3; ++c) fma8(acc + c * 8, p[g], vv[g * 3 + c]);
    fold_groups<24>(acc, r, bit3);
    const int i0 = (lane >> 5) * 12 + ((lane >> 4) & 1) * 6 + (bit3 ? 3 : 0);
#pragma unroll
    for (int k = 0; k < 3; ++k) L.raw[w][((i0 + k) >> 3) * 64 + sub * 8 + ((i0 + k) & 7)] = r[k];
  }
  wg_barrier();
  if (tid < 194) {
    float o = 0.f;
#pragma unroll
    for (int i = 0; i < 8; ++i) o += tid < DH ? L.raw[i][tid] : L.lw[i];
    putf(e1 + bid * 256 + tid, tag, tid == DH ? M : o);
  }
  wg_barrier();
  STAMP(4);

  {
    gu64* src = e1 + (size_t)(peer0 + 8 * w) * 256 + lane;
    const int o3 = lane < 2 ? 192 : 0;
    u64 g0 = ldg(src), g1 = ldg(src + 64), g2 = ldg(src + 128), g3 = ldg(src + o3);
    if (!__all(okf(g0, tag) && okf(g1, tag) && okf(g2, tag) && okf(g3, tag))) {
      wait_f(src - lane + 193, tag);
      STAMP(11);
      for (unsigned spins = 0; spins < SPIN_CAP; ++spins) {
        g0 = ldg(src); g1 = ldg(src + 64); g2 = ldg(src + 128); g3 = ldg(src + o3);
        if (__all(okf(g0, tag) && okf(g1, tag) && okf(g2, tag) && okf(g3, tag))) break;
        __builtin_amdgcn_s_sleep(1);
      }
    }
    STAMP(12);
    L.raw[w][lane] = valf(g0); L.raw[w][64 + lane] = valf(g1); L.raw[w][128 + lane] = valf(g2);
    if (lane < 2) L.raw[w][192 + lane] = valf(g3);
  }
  wg_barrier();
  STAMP(5);
  if (tid < 2 * DH) {
    const int bb = tid >= DH ? 1 : 0, d = tid - bb * DH;
    const float m0 = L.raw[bb * 4][192], m1v = L.raw[bb * 4 + 1][192], m2 = L.raw[bb * 4 + 2][192], m3 = L.raw[bb * 4 + 3][192];
    const float MM = fmaxf(fmaxf(m0, m1v), fmaxf(m2, m3));
    const float w0 = __expf(m0 - MM), w1 = __expf(m1v - MM), w2 = __expf(m2 - MM), w3 = __expf(m3 - MM);
    const float o = (w0 * L.raw[bb * 4][d] + w1 * L.raw[bb * 4 + 1][d]) + (w2 * L.raw[bb * 4 + 2][d] + w3 * L.raw[bb * 4 + 3][d]);
    const float l = (w0 * L.raw[bb * 4][193] + w1 * L.raw[bb * 4 + 1][193]) + (w2 * L.raw[bb * 4 + 2][193] + w3 * L.raw[bb * 4 + 3][193]);
    L.on[bb][d] = rbf(o * __builtin_amdgcn_rcpf(l));
  }
  wg_barrier();
  if (lane < 60) {
    const int r10 = lane / 10, c10 = lane - r10 * 10;
    float a0[8], a1[8];
#pragma unroll
    for (int e = 0; e < 8; ++e) { a0[e] = 0.f; a1[e] = 0.f; }
#pragma unroll
    for (int i = 0; i < 4; ++i) {
      const int row = w * 24 + i * 6 + r10;
      fma8(a0, L.on[0][row], wov[i]);
      fma8(a1, L.on[1][row], wov[i]);
    }
    float* dst = &L.part[w * 6 + r10][c10 * 16];
#pragma unroll
    for (int e = 0; e < 8; e += 2) *(f32x4*)(dst + 2 * e) = f32x4{a0[e], a1[e], a0[e + 1], a1[e + 1]};
  }
  wg_barrier();
  if (tid < 320) {
    const int o = tid >> 1, hf = (tid & 1) * 24;
    float t0 = 0.f, t1 = 0.f, t2 = 0.f, t3 = 0.f;
#pragma unroll
    for (int i = 0; i < 24; i += 4) { t0 += L.part[hf + i][o]; t1 += L.part[hf + i + 1][o]; t2 += L.part[hf + i + 2][o]; t3 += L.part[hf + i + 3][o]; }
    float t = (t0 + t1) + (t2 + t3);
    t += dpp<XOR1>(t);
    if ((tid & 1) == 0) putf(e2a + (size_t)h * (2 * D) + j * 160 + o, tag, t);
  }
  STAMP(6);

  float hres = 0.f;
  if (has_col) {
    gu64* src = e2a + (size_t)(lane >> 4) * (2 * D) + ccol * 2 + (lane & 15);
    u64 g[8];
    for (unsigned spins = 0; spins < SPIN_CAP; ++spins) {
      bool ok = true;
#pragma unroll
      for (int i = 0; i < 8; ++i) g[i] = ldg(src + (size_t)i * 4 * (2 * D));
#pragma unroll
      for (int i = 0; i < 8; ++i) ok = ok && okf(g[i], tag);
      if (__all(ok)) break;
      __builtin_amdgcn_s_sleep(1);
    }
    STAMP(13);
    float t = ((valf(g[0]) + valf(g[1])) + (valf(g[2]) + valf(g[3]))) + ((valf(g[4]) + valf(g[5])) + (valf(g[6]) + valf(g[7])));
    t = fold16(t, t);
    t = fold32(t, t);
    hres = rbf(bf2f(xres_raw) + rbf(t));
    const unsigned hb = f2bf(hres);
    const unsigned other = __builtin_amdgcn_mov_dpp(hb, XOR1, 0xf, 0xf, true);
    if ((lane & 1) == 0) {
      putu(e2b + (lane >> 4) * D + ccol + ((lane >> 1) & 7), tag, hb | (other << 16));
      putu(e2b + ((lane >> 4) + 4) * D + ccol + ((lane >> 1) & 7), tag, hb | (other << 16));
    }
    STAMP(14);
  }
  wg_barrier();
  STAMP(7);

  if (is_f1) {
    {
      const int o1 = tid < 128 ? 512 : 0;
      gu64* src = e2b + xcd * D + tid;
      if (SENT_E2B == 1) { for (unsigned spins = 0; spins < SPIN_CAP; ++spins) { if (oku(ldg(src - lane), tag)) break; __builtin_amdgcn_s_sleep(2); } }
      STAMP(15);
      u64 g0 = ldg(src), g1 = ldg(src + o1);
      for (unsigned spins = 0; !__all(oku(g0, tag) && oku(g1, tag)) && spins < SPIN_CAP; ++spins) {
        __builtin_amdgcn_s_sleep(SLEEP_N);
        g0 = ldg(src); g1 = ldg(src + o1);
      }
      STAMP(16);
      L.hv[0][tid] = lo2f((unsigned)g0); L.hv[1][tid] = hi2f((unsigned)g0);
      if (tid < 128) { L.hv[0][512 + tid] = lo2f((unsigned)g1); L.hv[1][512 + tid] = hi2f((unsigned)g1); }
    }
    wg_barrier();
    {
      STAMP(17);
      float m0, s0, m1v, s1;
      const int r0 = kq1 * 160 + (tid >> 3);
      float hr0[3], hr1[3];
      {
        f32x4 v[5];
#pragma unroll
        for (int i = 0; i < 5; ++i) v[i] = *(const f32x4*)&L.hv[lane >> 5][(lane & 31) * 4 + 128 * i];
#pragma unroll
        for (int i = 0; i < 3; ++i) { const int row = (i < 2 || tid < 256) ? r0 + 64 * i : 0; hr0[i] = L.hv[0][row]; hr1[i] = L.hv[1][row]; }
        const f32x4 sv = (v[0] + v[1]) + (v[2] + v[3]) + v[4];
        const float mean = half_sum((sv[0] + sv[1]) + (sv[2] + sv[3])) * (1.0f / D);
        f32x4 qv = {0.f, 0.f, 0.f, 0.f};
#pragma unroll
        for (int i = 0; i < 5; ++i) { const f32x4 d = v[i] - mean; qv += d * d; }
        const float var = half_sum((qv[0] + qv[1]) + (qv[2] + qv[3])) * (1.0f / D);
        const float mb = rbf(mean), rs = __builtin_amdgcn_rcpf(rbf(__builtin_amdgcn_sqrtf(rbf(rbf(var) + 1e-5f))));
        auto pm = __builtin_amdgcn_permlane32_swap(__float_as_uint(mb), __float_as_uint(mb), false, false);
        auto pr = __builtin_amdgcn_permlane32_swap(__float_as_uint(rs), __float_as_uint(rs), false, false);
        m0 = __uint_as_float(pm[0]); m1v = __uint_as_float(pm[1]); s0 = __uint_as_float(pr[0]); s1 = __uint_as_float(pr[1]);
      }
      float a[16], r[2];
#pragma unroll
      for (int e = 0; e < 16; ++e) a[e] = 0.f;
#pragma unroll
      for (int i = 0; i < 3; ++i) {
        const bool live = i < 2 || tid < 256;
        fma8(a, live ? ln_apply(hr0[i], m0, s0) : 0.f, f1w[i]);
        fma8(a + 8, live ? ln_apply(hr1[i], m1v, s1) : 0.f, f1w[i]);
      }
      fold_groups<16>(a, r, bit3);
      *(f32x2*)&L.red[w][(lane >> 5) * 64 + sub * 8 + ((lane >> 4) & 1) * 4 + (bit3 ? 2 : 0)] = f32x2{r[0], r[1]};
      STAMP(18);
    }
    wg_barrier();
    if (tid < 128) {
      float t = 0.f;
#pragma unroll
      for (int i = 0; i < 8; ++i) t += L.red[i][tid];
      putf(e3a + (size_t)((cb1 * 2 + m1) * 4 + kq1) * 128 + (tid & 63) * 2 + (tid >> 6), tag, t);
    }
  }
  STAMP(8);

  if (is_f2) {
    gu64* src = e3a + (size_t)(kp2 * 8 + sub) * 128 + (tid >> 3) * 2;
    if (SENT_E3A == 1) {
      gu64* sent = e3a + (size_t)(kp2 * 8 + sub) * 128 + w * 16;
      for (unsigned spins = 0; spins < SPIN_CAP; ++spins) { if (__all(okf(ldg(sent), tag))) break; __builtin_amdgcn_s_sleep(2); }
    } else if (SENT_E3A == 2) {
      if (w == 0) {
        gu64* sent = e3a + (size_t)(kp2 * 8 + sub) * 128 + (grp & 1) * 127;
        for (unsigned spins = 0; spins < SPIN_CAP; ++spins) { if (__all(okf(ldg(sent), tag))) break; __builtin_amdgcn_s_sleep(1); }
      }
      wg_barrier();
    }
    STAMP(19);
    u64 g0 = ldg(src), g1 = ldg(src + 1);
    for (unsigned spins = 0; !__all(okf(g0, tag) && okf(g1, tag)) && spins < SPIN_CAP; ++spins) {
      __builtin_amdgcn_s_sleep(SLEEP_N);
      g0 = ldg(src); g1 = ldg(src + 1);
    }
    STAMP(20);
    float t0 = valf(g0), t1 = valf(g1);
    t0 += dpp<XOR1>(t0); t1 += dpp<XOR1>(t1);
    t0 += dpp<XOR2>(t0); t1 += dpp<XOR2>(t1);
    const float o0 = dpp<HMIRROR>(t0), o1 = dpp<HMIRROR>(t1);
    const bool lowq = sub < 4;
    const float g_0 = rbf(lowq ? t0 : o0), g_1 = rbf(lowq ? t1 : o1), u_0 = rbf(lowq ? o0 : t0), u_1 = rbf(lowq ? o1 : t1);
    const float act0 = rbf(rbf(gelu_tanh(g_0)) * u_0), act1 = rbf(rbf(gelu_tanh(g_1)) * u_1);
    float a[16], r[2];
#pragma unroll
    for (int e = 0; e < 16; ++e) a[e] = 0.f;
    fma8(a, act0, f2w);
    fma8(a + 8, act1, f2w);
    fold_groups<16>(a, r, bit3);
    *(f32x2*)&L.part[w][(lane >> 5) * 64 + sub * 8 + ((lane >> 4) & 1) * 4 + (bit3 ? 2 : 0)] = f32x2{r[0], r[1]};
    STAMP(21);
    wg_barrier();
    if (tid < 128) {
      float t2 = 0.f;
#pragma unroll
      for (int i = 0; i < 8; ++i) t2 += L.part[i][tid];
      putf(e3b + (size_t)kp2 * (2 * D) + (cb2 * 64 + (tid & 63)) * 2 + (tid >> 6), tag, t2);
    }
  }
  STAMP(9);

  if (has_col) {
    gu64* src = e3b + (size_t)(lane >> 4) * (2 * D) + ccol * 2 + (lane & 15);
    u64 g[6];
    for (unsigned spins = 0; spins < SPIN_CAP; ++spins) {
      bool ok = true;
#pragma unroll
      for (int i = 0; i < 6; ++i) g[i] = ldg(src + (size_t)i * 4 * (2 * D));
#pragma unroll
      for (int i = 0; i < 6; ++i) ok = ok && okf(g[i], tag);
      if (__all(ok)) break;
      __builtin_amdgcn_s_sleep(1);
    }
    STAMP(22);
    float t = ((valf(g[0]) + valf(g[1])) + (valf(g[2]) + valf(g[3]))) + (valf(g[4]) + valf(g[5]));
    t = fold16(t, t);
    t = fold32(t, t);
    if (lane < 16) out[(lane & 1) * D + ccol + (lane >> 1)] = f2bf(hres + rbf(t));
    if (ccol == 0 && lane == 0) __hip_atomic_store((gu64*)(ws + WS_EPOCH), tag, RLX_AGENT);
  }
  STAMP(10);
}
}

extern "C" void kernel_launch(void* const* d_in, const int* in_sizes, int n_in, void* d_out, int out_size, void* d_ws,
                              size_t ws_size, hipStream_t stream) {
  const unsigned short* x = (const unsigned short*)d_in[0];
  const unsigned short* kc = (const unsigned short*)d_in[1];
  const unsigned short* vc = (const unsigned short*)d_in[2];
  const unsigned short* wq = (const unsigned short*)d_in[3];
  const unsigned short* wo = (const unsigned short*)d_in[4];
  const unsigned short* wg = (const unsigned short*)d_in[6];
  const unsigned short* wu = (const unsigned short*)d_in[7];
  const unsigned short* wd = (const unsigned short*)d_in[8];
  fused_kernel<<<dim3(NWG), dim3(NTHR), 0, stream>>>(x, kc, vc, wq, wo, wg, wu, wd, (unsigned short*)d_out,
                                                      (unsigned char*)d_ws);
}
```

```cpp
#define DIAG_K 0
#include <hip/hip_runtime.h>
#include <hip/hip_bf16.h>
#include <stdint.h>

#define GAS __attribute__((address_space(1)))
typedef unsigned v4u __attribute__((ext_vector_type(4)));
typedef float f32x4 __attribute__((ext_vector_type(4)));
typedef float f32x2 __attribute__((ext_vector_type(2)));
typedef __bf16 bf16x2 __attribute__((ext_vector_type(2)));
typedef unsigned long long u64;
typedef GAS u64 gu64;
#define RLX_AGENT __ATOMIC_RELAXED, __HIP_MEMORY_SCOPE_AGENT
#define RLX_WG __ATOMIC_RELAXED, __HIP_MEMORY_SCOPE_WORKGROUP

#define POLL_BT 5
#define ST_NT 0
#define WO_POS 0
#define FFN_POS 0
#define GATE_BT 1
#define SENT_E3A 2
#define SENT_E2B 1
#define SLEEP_N 1
namespace {
constexpr int D = 640, HQ = 32, DH = 192, SK = 8192, WIN = 2048, FF = 1536;
constexpr int QCOLS = HQ * DH;
constexpr int NWG = 256, NTHR = 512;
constexpr int KEYS_WG = WIN / 4;
constexpr unsigned SPIN_CAP = 1u << 20;

constexpr size_t WS_EPOCH = 0;
constexpr size_t WS_E0 = 4096;
constexpr size_t WS_E1 = WS_E0 + (size_t)NWG * 384 * 8;
constexpr size_t WS_E2A = WS_E1 + (size_t)NWG * 256 * 8;
constexpr size_t WS_E2B = WS_E2A + (size_t)D * 64 * 8;
constexpr size_t WS_E3A = WS_E2B + (size_t)8 * D * 8;
constexpr size_t WS_E3B = WS_E3A + (size_t)FF * 16 * 8;

__device__ __forceinline__ float bf2f(unsigned short u) { return __uint_as_float((unsigned)u << 16); }
__device__ __forceinline__ float lo2f(unsigned u) { return __uint_as_float(u << 16); }
__device__ __forceinline__ float hi2f(unsigned u) { return __uint_as_float(u & 0xffff0000u); }
__device__ __forceinline__ unsigned short f2bf(float f) { return __builtin_bit_cast(unsigned short, (__bf16)f); }
__device__ __forceinline__ float rbf(float f) { return (float)(__bf16)f; }
__device__ __forceinline__ float dot2(unsigned k, unsigned q, float acc) {
  return __builtin_amdgcn_fdot2_f32_bf16(__builtin_bit_cast(bf16x2, k), __builtin_bit_cast(bf16x2, q), acc, false);
}
template <int CTRL> __device__ __forceinline__ float dpp(float x) {
  return __int_as_float(__builtin_amdgcn_mov_dpp(__float_as_int(x), CTRL, 0xf, 0xf, true));
}
constexpr int XOR1 = 0xB1, XOR2 = 0x4E, HMIRROR = 0x141, ROR8 = 0x128, QP0 = 0x00, QP2 = 0xAA;
__device__ __forceinline__ float fold32(float a, float b) {
  auto s = __builtin_amdgcn_permlane32_swap(__float_as_uint(a), __float_as_uint(b), false, false);
  return __uint_as_float(s[0]) + __uint_as_float(s[1]);
}
__device__ __forceinline__ float fold16(float a, float b) {
  auto s = __builtin_amdgcn_permlane16_swap(__float_as_uint(a), __float_as_uint(b), false, false);
  return __uint_as_float(s[0]) + __uint_as_float(s[1]);
}
__device__ __forceinline__ float fold8(float a, float b, bool bit3) {
  const float t = a + dpp<ROR8>(a), u = b + dpp<ROR8>(b);
  return bit3 ? u : t;
}
template <int N> __device__ __forceinline__ void fold_groups(const float (&v)[N], float (&r)[N / 8], bool bit3) {
  float a[N / 2], c[N / 4];
#pragma unroll
  for (int i = 0; i < N / 2; ++i) a[i] = fold32(v[i], v[i + N / 2]);
#pragma unroll
  for (int i = 0; i < N / 4; ++i) c[i] = fold16(a[i], a[i + N / 4]);
#pragma unroll
  for (int i = 0; i < N / 8; ++i) r[i] = fold8(c[i], c[i + N / 8], bit3);
}
__device__ __forceinline__ float swap16_max(float x) {
  auto s = __builtin_amdgcn_permlane16_swap(__float_as_uint(x), __float_as_uint(x), false, false);
  return fmaxf(__uint_as_float(s[0]), __uint_as_float(s[1]));
}
__device__ __forceinline__ float swap32_max(float x) {
  auto s = __builtin_amdgcn_permlane32_swap(__float_as_uint(x), __float_as_uint(x), false, false);
  return fmaxf(__uint_as_float(s[0]), __uint_as_float(s[1]));
}
__device__ __forceinline__ float grp_sum(float x) { x += dpp<ROR8>(x); x = fold16(x, x); return fold32(x, x); }
__device__ __forceinline__ float grp_max(float x) { x = fmaxf(x, dpp<ROR8>(x)); x = swap16_max(x); return swap32_max(x); }
__device__ __forceinline__ float oct_sum(float x) { x += dpp<XOR1>(x); x += dpp<XOR2>(x); x += dpp<HMIRROR>(x); return x; }
__device__ __forceinline__ float wave_sum(float x) { return grp_sum(oct_sum(x)); }
__device__ __forceinline__ float half_sum(float x) { x = oct_sum(x); x += dpp<ROR8>(x); return fold16(x, x); }

__device__ __forceinline__ void wg_barrier() {
  asm volatile("s_waitcnt lgkmcnt(0)" ::: "memory");
  __builtin_amdgcn_s_barrier();
  asm volatile("" ::: "memory");
}
__device__ __forceinline__ void lds_wait() { asm volatile("s_waitcnt lgkmcnt(0)" ::: "memory"); }
__device__ __forceinline__ void stg(gu64* g, u64 v) {
  __hip_atomic_store(g, v, RLX_AGENT);
}
__device__ __forceinline__ void putf(gu64* g, u64 tag, float v) { stg(g, (tag << 24) | (u64)(__float_as_uint(v) >> 8)); }
__device__ __forceinline__ u64 ldg(gu64* g) { return __hip_atomic_load(g, RLX_AGENT); }
__device__ __forceinline__ bool okf(u64 x, u64 tag) { return (x >> 24) == tag; }
__device__ __forceinline__ float valf(u64 x) { return __uint_as_float(((unsigned)x) << 8); }
__device__ __forceinline__ void putu(gu64* g, u64 tag, unsigned v) { stg(g, (tag << 32) | (u64)v); }
__device__ __forceinline__ bool oku(u64 x, u64 tag) { return (unsigned)(x >> 32) == (unsigned)tag; }
__device__ __forceinline__ void wait_f(gu64* g, u64 tag) {
  for (unsigned spins = 0; spins < SPIN_CAP; ++spins) {
    if (okf(ldg(g), tag)) break;
    __builtin_amdgcn_s_sleep(2);
  }
}
__device__ __forceinline__ v4u ldnt(const void* p) { return __builtin_nontemporal_load((const GAS v4u*)p); }

__device__ __forceinline__ void ln_stats(const float (&v)[10], float& mean_bf, float& rsd) {
  float s = 0.f;
#pragma unroll
  for (int i = 0; i < 10; ++i) s += v[i];
  const float mean = wave_sum(s) * (1.0f / D);
  float q = 0.f;
#pragma unroll
  for (int i = 0; i < 10; ++i) { const float d = v[i] - mean; q = fmaf(d, d, q); }
  const float var = wave_sum(q) * (1.0f / D);
  mean_bf = rbf(mean);
  rsd = __builtin_amdgcn_rcpf(rbf(__builtin_amdgcn_sqrtf(rbf(rbf(var) + 1e-5f))));
}
__device__ __forceinline__ float ln_apply(float x, float mean_bf, float rsd) { return rbf(rbf(x - mean_bf) * rsd); }
__device__ __forceinline__ void fma8(float* a, float s, v4u t) {
#pragma unroll
  for (int e = 0; e < 4; ++e) { a[2 * e] = fmaf(s, lo2f(t[e]), a[2 * e]); a[2 * e + 1] = fmaf(s, hi2f(t[e]), a[2 * e + 1]); }
}
__device__ __forceinline__ float gelu_tanh(float g) {
  const float y = 0.7978845608028654f * (g + 0.044715f * g * g * g);
  const float t = 1.0f - 2.0f * __builtin_amdgcn_rcpf(__expf(2.0f * y) + 1.0f);
  return 0.5f * g * (1.0f + t);
}

#define STAMP(i) do { if ((i) == 0 || (i) == DIAG_K) stv[i] = __builtin_amdgcn_s_memrealtime(); } while (0)

struct Lds {
  float xn[5][2][80];
  float part[48][160];
  float raw[8][200];
  float on[2][DH];
  float hv[2][D];
  float mw[8], lw[8];
  float red[8][128];
  unsigned short qs[DH];
  unsigned pcnt, qcnt, qflag;
};

__global__ void __launch_bounds__(NTHR, 2)
fused_kernel(const unsigned short* __restrict__ x, const unsigned short* __restrict__ kc, const unsigned short* __restrict__ vc,
             const unsigned short* __restrict__ wq, const unsigned short* __restrict__ wo, const unsigned short* __restrict__ wg,
             const unsigned short* __restrict__ wu, const unsigned short* __restrict__ wd, unsigned short* __restrict__ out,
             unsigned char* ws) {
  __shared__ __attribute__((aligned(16))) Lds L;
  const int tid = threadIdx.x, lane = tid & 63, w = __builtin_amdgcn_readfirstlane(tid >> 6);
  const int bid = blockIdx.x, xcd = bid & 7, idx = bid >> 3;
  const int h = xcd * 4 + (idx >> 3), j = idx & 7, b = j >> 2, s = j & 3;
  const int grp = lane >> 3, sub = lane & 7;
  const bool bit3 = (lane & 8) != 0;
  const int peer0 = xcd | ((idx & ~7) << 3);
  const bool is_f1 = bid < 192;
  const int cb1 = bid % 24, m1 = (bid / 24) & 1, kq1 = bid / 48;
  const bool is_f2 = bid < 240;
  const int cb2 = bid % 10, kp2 = bid / 10;
  const bool has_col = bid >= 176 && w == 0;
  const int ccol = (bid >= 176 ? bid - 176 : 0) * 8;

  gu64* e0 = (gu64*)(ws + WS_E0);
  gu64* e1 = (gu64*)(ws + WS_E1);
  gu64* e2a = (gu64*)(ws + WS_E2A);
  gu64* e2b = (gu64*)(ws + WS_E2B);
  gu64* e3a = (gu64*)(ws + WS_E3A);
  gu64* e3b = (gu64*)(ws + WS_E3B);

  u64 stv[32];
#pragma unroll
  for (int i = 0; i < 32; ++i) stv[i] = 0;
  STAMP(0);
  const u64 tag = (ldg((gu64*)(ws + WS_EPOCH)) + 1ull) & 0xFFFFFFFFFFull;
  const unsigned short xres_raw = x[(lane & 1) * D + ccol + ((lane >> 1) & 7)];
  unsigned xr[2][5];
  v4u wqv[6];
  if (w < 5) {
#pragma unroll
    for (int bb = 0; bb < 2; ++bb)
#pragma unroll
      for (int i = 0; i < 5; ++i) xr[bb][i] = *(const GAS unsigned*)(x + bb * D + 2 * (lane + 64 * i));
    const unsigned short* p = wq + (size_t)(j * 80 + w * 16 + grp) * QCOLS + h * DH + sub * 8;
#pragma unroll
    for (int g = 0; g < 2; ++g)
#pragma unroll
      for (int c = 0; c < 3; ++c) wqv[g * 3 + c] = ldnt(p + (size_t)g * 8 * QCOLS + c * 64);
  }
  if (tid == 0) { L.pcnt = 0u; L.qcnt = 0u; L.qflag = 0u; }
  __builtin_amdgcn_sched_barrier(0);
  wg_barrier();
  const size_t kvoff = ((size_t)(b * HQ + h) * SK + (SK - WIN) + s * KEYS_WG + w * 64 + grp) * DH + sub * 8;
  v4u kv[24], vv[24];
  {
    const bool poller = w >= 1 && w <= 3;
    const int p0 = (w - 1) * 3, np = w == 3 ? 2 : 3;
    gu64* src = e0 + (size_t)(peer0 + 8 * (poller ? p0 : 0)) * 384 + b * DH + lane;
    u64 g[3][3];
#pragma unroll
    for (int bt = 0; bt < 8; ++bt) {
      if (bt == GATE_BT) { while (__builtin_amdgcn_readfirstlane(__hip_atomic_load(&L.pcnt, RLX_WG)) < 100u) __builtin_amdgcn_s_sleep(1); }
      if (bt == POLL_BT && poller) {
#pragma unroll
        for (int i = 0; i < 3; ++i)
#pragma unroll
          for (int k = 0; k < 3; ++k) g[i][k] = ldg(src + (size_t)(i < np ? i : 0) * 8 * 384 + 64 * k);
      }
      __builtin_amdgcn_sched_barrier(0);
#pragma unroll
      for (int gg = 0; gg < 2; ++gg)
#pragma unroll
        for (int c = 0; c < 3; ++c) {
          if (bt < 4) kv[(bt * 2 + gg) * 3 + c] = ldnt(kc + kvoff + (bt * 2 + gg) * 8 * DH + c * 64);
          else vv[((bt - 4) * 2 + gg) * 3 + c] = ldnt(vc + kvoff + ((bt - 4) * 2 + gg) * 8 * DH + c * 64);
        }
      __builtin_amdgcn_sched_barrier(0);
      if (bt == GATE_BT - 1 && w < 5) {
#pragma unroll
        for (int bb = 0; bb < 2; ++bb) {
          float xv[10];
#pragma unroll
          for (int i = 0; i < 5; ++i) { xv[2 * i] = lo2f(xr[bb][i]); xv[2 * i + 1] = hi2f(xr[bb][i]); }
          float mb, rs;
          ln_stats(xv, mb, rs);
#pragma unroll
          for (int i = 0; i < 5; ++i) {
            const int e = 2 * (lane + 64 * i) - j * 80;
            if (e >= 0 && e < 80) *(f32x2*)&L.xn[w][bb][e] = f32x2{ln_apply(xv[2 * i], mb, rs), ln_apply(xv[2 * i + 1], mb, rs)};
          }
        }
        lds_wait();
        float qa[48], qr[6];
#pragma unroll
        for (int i = 0; i < 48; ++i) qa[i] = 0.f;
#pragma unroll
        for (int g = 0; g < 2; ++g) {
          const float x0 = L.xn[w][0][w * 16 + g * 8 + grp], x1 = L.xn[w][1][w * 16 + g * 8 + grp];
#pragma unroll
          for (int c = 0; c < 3; ++c) { fma8(qa + c * 8, x0, wqv[g * 3 + c]); fma8(qa + 24 + c * 8, x1, wqv[g * 3 + c]); }
        }
        fold_groups<48>(qa, qr, bit3);
        {
          const int i0 = ((lane >> 4) & 1) * 12 + (bit3 ? 6 : 0);
          float* dst = &L.part[0][0] + w * 384 + (lane >> 5) * DH + sub * 8;
#pragma unroll
          for (int k = 0; k < 6; ++k) dst[((i0 + k) >> 3) * 64 + ((i0 + k) & 7)] = qr[k];
        }
        lds_wait();
        unsigned old = 0;
        if (lane == 0) old = __hip_atomic_fetch_add(&L.pcnt, 1u, RLX_WG);
        if (__builtin_amdgcn_readfirstlane(old) == 4u) {
#pragma unroll
          for (int k = 0; k < 6; ++k) {
            const float* sp = &L.part[0][0] + lane + 64 * k;
            putf(e0 + bid * 384 + lane + 64 * k, tag, (sp[0] + sp[384]) + (sp[768] + sp[1152]) + sp[1536]);
          }
          __builtin_amdgcn_sched_barrier(0);
          if (lane == 0) __hip_atomic_store(&L.pcnt, 100u, RLX_WG);
        }
        STAMP(1);
      }
    }
    if (poller) {
      bool ok = true;
#pragma unroll
      for (int i = 0; i < 3; ++i)
#pragma unroll
        for (int k = 0; k < 3; ++k) ok = ok && okf(g[i][k], tag);
      if (__all(ok)) {
#pragma unroll
        for (int i = 0; i < 3; ++i)
          if (i < np) {
#pragma unroll
            for (int k = 0; k < 3; ++k) L.raw[p0 + i][64 * k + lane] = valf(g[i][k]);
          }
      } else {
        for (int i = 0; i < np; ++i) {
          gu64* sp = src + (size_t)i * 8 * 384;
          wait_f(sp - lane, tag);
          for (unsigned spins = 0; spins < SPIN_CAP; ++spins) {
            const u64 r0 = ldg(sp), r1 = ldg(sp + 64), r2 = ldg(sp + 128);
            L.raw[p0 + i][lane] = valf(r0); L.raw[p0 + i][64 + lane] = valf(r1); L.raw[p0 + i][128 + lane] = valf(r2);
            if (__all(okf(r0, tag) && okf(r1, tag) && okf(r2, tag))) break;
            __builtin_amdgcn_s_sleep(2);
          }
        }
      }
      lds_wait();
      unsigned old = 0;
      if (lane == 0) old = __hip_atomic_fetch_add(&L.qcnt, 1u, RLX_WG);
      if (__builtin_amdgcn_readfirstlane(old) == 2u) {
#pragma unroll
        for (int k = 0; k < 3; ++k) {
          float q = 0.f;
#pragma unroll
          for (int i = 0; i < 8; ++i) q += L.raw[i][lane + 64 * k];
          L.qs[lane + 64 * k] = f2bf(q);
        }
        lds_wait();
        if (lane == 0) __hip_atomic_store(&L.qflag, 1u, RLX_WG);
      }
    }
    while (__builtin_amdgcn_readfirstlane(__hip_atomic_load(&L.qflag, RLX_WG)) == 0u) __builtin_amdgcn_s_sleep(1);
    asm volatile("" ::: "memory");
  }
  STAMP(2);
  v4u qp[3];
#pragma unroll
  for (int c = 0; c < 3; ++c) qp[c] = *(const v4u*)&L.qs[c * 64 + sub * 8];

  float sc[8];
  {
#pragma unroll
    for (int g = 0; g < 8; ++g) {
      float a = 0.f;
#pragma unroll
      for (int c = 0; c < 3; ++c)
#pragma unroll
        for (int e = 0; e < 4; ++e) a = dot2(kv[g * 3 + c][e], qp[c][e], a);
      a = oct_sum(a);
      sc[g] = rbf(rbf(a) * 0.07216878364870322f);
    }
    float m = sc[0];
#pragma unroll
    for (int g = 1; g < 8; ++g) m = fmaxf(m, sc[g]);
    m = grp_max(m);
    if (lane == 0) L.mw[w] = m;
  }
  STAMP(3);

  v4u wov[4], f1w[3], f2w;
  {
    const int r10 = lane / 10, c10 = lane - r10 * 10;
    const unsigned short* p = wo + (size_t)(h * DH + w * 24 + (lane < 60 ? r10 : 0)) * D + j * 80 + (lane < 60 ? c10 : 0) * 8;
#pragma unroll
    for (int i = 0; i < 4; ++i) wov[i] = ldnt(p + (size_t)i * 6 * D);
  }
  __builtin_amdgcn_sched_barrier(0);
  {
    const unsigned short* p1 = (m1 ? wu : wg) + (size_t)((is_f1 ? kq1 : 0) * 160 + (tid >> 3)) * FF + cb1 * 64 + sub * 8;
    f1w[0] = ldnt(p1);
    f1w[1] = ldnt(p1 + (size_t)64 * FF);
    f1w[2] = ldnt(p1 + (size_t)(tid < 256 ? 128 : 0) * FF);
    f2w = ldnt(wd + (size_t)((is_f2 ? kp2 : 0) * 64 + (tid >> 3)) * D + cb2 * 64 + sub * 8);
  }
  __builtin_amdgcn_sched_barrier(0);
  __builtin_amdgcn_sched_barrier(0);
  wg_barrier();

  float M;
  {
    const f32x4 ma = *(const f32x4*)&L.mw[0], mb = *(const f32x4*)&L.mw[4];
    M = fmaxf(fmaxf(fmaxf(ma[0], ma[1]), fmaxf(ma[2], ma[3])), fmaxf(fmaxf(mb[0], mb[1]), fmaxf(mb[2], mb[3])));
    float l = 0.f, p[8];
#pragma unroll
    for (int g = 0; g < 8; ++g) { p[g] = __expf(sc[g] - M); l += p[g]; }
    l = grp_sum(l);
    if (lane == 0) L.lw[w] = l;
    float acc[24], r[3];
#pragma unroll
    for (int i = 0; i < 24; ++i) acc[i] = 0.f;
#pragma unroll
    for (int g = 0; g < 8; ++g)
#pragma unroll
      for (int c = 0; c < 3; ++c) fma8(acc + c * 8, p[g], vv[g * 3 + c]);
    fold_groups<24>(acc, r, bit3);
    const int i0 = (lane >> 5) * 12 + ((lane >> 4) & 1) * 6 + (bit3 ? 3 : 0);
#pragma unroll
    for (int k = 0; k < 3; ++k) L.raw[w][((i0 + k) >> 3) * 64 + sub * 8 + ((i0 + k) & 7)] = r[k];
  }
  wg_barrier();
  if (tid < 194) {
    float o = 0.f;
#pragma unroll
    for (int i = 0; i < 8; ++i) o += tid < DH ? L.raw[i][tid] : L.lw[i];
    putf(e1 + bid * 256 + tid, tag, tid == DH ? M : o);
  }
  wg_barrier();
  STAMP(4);

  {
    gu64* src = e1 + (size_t)(peer0 + 8 * w) * 256 + lane;
    const int o3 = lane < 2 ? 192 : 0;
    u64 g0 = ldg(src), g1 = ldg(src + 64), g2 = ldg(src + 128), g3 = ldg(src + o3);
    if (!__all(okf(g0, tag) && okf(g1, tag) && okf(g2, tag) && okf(g3, tag))) {
      wait_f(src - lane + 193, tag);
      STAMP(11);
      for (unsigned spins = 0; spins < SPIN_CAP; ++spins) {
        g0 = ldg(src); g1 = ldg(src + 64); g2 = ldg(src + 128); g3 = ldg(src + o3);
        if (__all(okf(g0, tag) && okf(g1, tag) && okf(g2, tag) && okf(g3, tag))) break;
        __builtin_amdgcn_s_sleep(1);
      }
    }
    STAMP(12);
    L.raw[w][lane] = valf(g0); L.raw[w][64 + lane] = valf(g1); L.raw[w][128 + lane] = valf(g2);
    if (lane < 2) L.raw[w][192 + lane] = valf(g3);
  }
  wg_barrier();
  STAMP(5);
  if (tid < 2 * DH) {
    const int bb = tid >= DH ? 1 : 0, d = tid - bb * DH;
    const float m0 = L.raw[bb * 4][192], m1v = L.raw[bb * 4 + 1][192], m2 = L.raw[bb * 4 + 2][192], m3 = L.raw[bb * 4 + 3][192];
    const float MM = fmaxf(fmaxf(m0, m1v), fmaxf(m2, m3));
    const float w0 = __expf(m0 - MM), w1 = __expf(m1v - MM), w2 = __expf(m2 - MM), w3 = __expf(m3 - MM);
    const float o = (w0 * L.raw[bb * 4][d] + w1 * L.raw[bb * 4 + 1][d]) + (w2 * L.raw[bb * 4 + 2][d] + w3 * L.raw[bb * 4 + 3][d]);
    const float l = (w0 * L.raw[bb * 4][193] + w1 * L.raw[bb * 4 + 1][193]) + (w2 * L.raw[bb * 4 + 2][193] + w3 * L.raw[bb * 4 + 3][193]);
    L.on[bb][d] = rbf(o * __builtin_amdgcn_rcpf(l));
  }
  wg_barrier();
  if (lane < 60) {
    const int r10 = lane / 10, c10 = lane - r10 * 10;
    float a0[8], a1[8];
#pragma unroll
    for (int e = 0; e < 8; ++e) { a0[e] = 0.f; a1[e] = 0.f; }
#pragma unroll
    for (int i = 0; i < 4; ++i) {
      const int row = w * 24 + i * 6 + r10;
      fma8(a0, L.on[0][row], wov[i]);
      fma8(a1, L.on[1][row], wov[i]);
    }
    float* dst = &L.part[w * 6 + r10][c10 * 16];
#pragma unroll
    for (int e = 0; e < 8; e += 2) *(f32x4*)(dst + 2 * e) = f32x4{a0[e], a1[e], a0[e + 1], a1[e + 1]};
  }
  wg_barrier();
  if (tid < 320) {
    const int o = tid >> 1, hf = (tid & 1) * 24;
    float t0 = 0.f, t1 = 0.f, t2 = 0.f, t3 = 0.f;
#pragma unroll
    for (int i = 0; i < 24; i += 4) { t0 += L.part[hf + i][o]; t1 += L.part[hf + i + 1][o]; t2 += L.part[hf + i + 2][o]; t3 += L.part[hf + i + 3][o]; }
    float t = (t0 + t1) + (t2 + t3);
    t += dpp<XOR1>(t);
    if ((tid & 1) == 0) putf(e2a + (size_t)h * (2 * D) + j * 160 + o, tag, t);
  }
  STAMP(6);

  float hres = 0.f;
  if (has_col) {
    gu64* src = e2a + (size_t)(lane >> 4) * (2 * D) + ccol * 2 + (lane & 15);
    u64 g[8];
    for (unsigned spins = 0; spins < SPIN_CAP; ++spins) {
      bool ok = true;
#pragma unroll
      for (int i = 0; i < 8; ++i) g[i] = ldg(src + (size_t)i * 4 * (2 * D));
#pragma unroll
      for (int i = 0; i < 8; ++i) ok = ok && okf(g[i], tag);
      if (__all(ok)) break;
      __builtin_amdgcn_s_sleep(1);
    }
    STAMP(13);
    float t = ((valf(g[0]) + valf(g[1])) + (valf(g[2]) + valf(g[3]))) + ((valf(g[4]) + valf(g[5])) + (valf(g[6]) + valf(g[7])));
    t = fold16(t, t);
    t = fold32(t, t);
    hres = rbf(bf2f(xres_raw) + rbf(t));
    const unsigned hb = f2bf(hres);
    const unsigned other = __builtin_amdgcn_mov_dpp(hb, XOR1, 0xf, 0xf, true);
    if ((lane & 1) == 0) {
      putu(e2b + (lane >> 4) * D + ccol + ((lane >> 1) & 7), tag, hb | (other << 16));
      putu(e2b + ((lane >> 4) + 4) * D + ccol + ((lane >> 1) & 7), tag, hb | (other << 16));
    }
    STAMP(14);
  }
  wg_barrier();
  STAMP(7);

  if (is_f1) {
    {
      const int o1 = tid < 128 ? 512 : 0;
      gu64* src = e2b + xcd * D + tid;
      if (SENT_E2B == 1) { for (unsigned spins = 0; spins < SPIN_CAP; ++spins) { if (oku(ldg(src - lane), tag)) break; __builtin_amdgcn_s_sleep(2); } }
      STAMP(15);
      u64 g0 = ldg(src), g1 = ldg(src + o1);
      for (unsigned spins = 0; !__all(oku(g0, tag) && oku(g1, tag)) && spins < SPIN_CAP; ++spins) {
        __builtin_amdgcn_s_sleep(SLEEP_N);
        g0 = ldg(src); g1 = ldg(src + o1);
      }
      STAMP(16);
      L.hv[0][tid] = lo2f((unsigned)g0); L.hv[1][tid] = hi2f((unsigned)g0);
      if (tid < 128) { L.hv[0][512 + tid] = lo2f((unsigned)g1); L.hv[1][512 + tid] = hi2f((unsigned)g1); }
    }
    wg_barrier();
    {
      STAMP(17);
      float m0, s0, m1v, s1;
      const int r0 = kq1 * 160 + (tid >> 3);
      float hr0[3], hr1[3];
      {
        f32x4 v[5];
#pragma unroll
        for (int i = 0; i < 5; ++i) v[i] = *(const f32x4*)&L.hv[lane >> 5][(lane & 31) * 4 + 128 * i];
#pragma unroll
        for (int i = 0; i < 3; ++i) { const int row = (i < 2 || tid < 256) ? r0 + 64 * i : 0; hr0[i] = L.hv[0][row]; hr1[i] = L.hv[1][row]; }
        const f32x4 sv = (v[0] + v[1]) + (v[2] + v[3]) + v[4];
        const float mean = half_sum((sv[0] + sv[1]) + (sv[2] + sv[3])) * (1.0f / D);
        f32x4 qv = {0.f, 0.f, 0.f, 0.f};
#pragma unroll
        for (int i = 0; i < 5; ++i) { const f32x4 d = v[i] - mean; qv += d * d; }
        const float var = half_sum((qv[0] + qv[1]) + (qv[2] + qv[3])) * (1.0f / D);
        const float mb = rbf(mean), rs = __builtin_amdgcn_rcpf(rbf(__builtin_amdgcn_sqrtf(rbf(rbf(var) + 1e-5f))));
        auto pm = __builtin_amdgcn_permlane32_swap(__float_as_uint(mb), __float_as_uint(mb), false, false);
        auto pr = __builtin_amdgcn_permlane32_swap(__float_as_uint(rs), __float_as_uint(rs), false, false);
        m0 = __uint_as_float(pm[0]); m1v = __uint_as_float(pm[1]); s0 = __uint_as_float(pr[0]); s1 = __uint_as_float(pr[1]);
      }
      float a[16], r[2];
#pragma unroll
      for (int e = 0; e < 16; ++e) a[e] = 0.f;
#pragma unroll
      for (int i = 0; i < 3; ++i) {
        const bool live = i < 2 || tid < 256;
        fma8(a, live ? ln_apply(hr0[i], m0, s0) : 0.f, f1w[i]);
        fma8(a + 8, live ? ln_apply(hr1[i], m1v, s1) : 0.f, f1w[i]);
      }
      fold_groups<16>(a, r, bit3);
      *(f32x2*)&L.red[w][(lane >> 5) * 64 + sub * 8 + ((lane >> 4) & 1) * 4 + (bit3 ? 2 : 0)] = f32x2{r[0], r[1]};
      STAMP(18);
    }
    wg_barrier();
    if (tid < 128) {
      float t = 0.f;
#pragma unroll
      for (int i = 0; i < 8; ++i) t += L.red[i][tid];
      putf(e3a + (size_t)((cb1 * 2 + m1) * 4 + kq1) * 128 + (tid & 63) * 2 + (tid >> 6), tag, t);
    }
  }
  STAMP(8);

  if (is_f2) {
    gu64* src = e3a + (size_t)(kp2 * 8 + sub) * 128 + (tid >> 3) * 2;
    if (SENT_E3A == 1) {
      gu64* sent = e3a + (size_t)(kp2 * 8 + sub) * 128 + w * 16;
      for (unsigned spins = 0; spins < SPIN_CAP; ++spins) { if (__all(okf(ldg(sent), tag))) break; __builtin_amdgcn_s_sleep(2); }
    } else if (SENT_E3A == 2) {
      if (w == 0) {
        gu64* sent = e3a + (size_t)(kp2 * 8 + sub) * 128 + (grp & 1) * 127;
        for (unsigned spins = 0; spins < SPIN_CAP; ++spins) { if (__all(okf(ldg(sent), tag))) break; __builtin_amdgcn_s_sleep(1); }
      }
      wg_barrier();
    }
    STAMP(19);
    u64 g0 = ldg(src), g1 = ldg(src + 1);
    for (unsigned spins = 0; !__all(okf(g0, tag) && okf(g1, tag)) && spins < SPIN_CAP; ++spins) {
      __builtin_amdgcn_s_sleep(SLEEP_N);
      g0 = ldg(src); g1 = ldg(src + 1);
    }
    STAMP(20);
    float t0 = valf(g0), t1 = valf(g1);
    t0 += dpp<XOR1>(t0); t1 += dpp<XOR1>(t1);
    t0 += dpp<XOR2>(t0); t1 += dpp<XOR2>(t1);
    const float o0 = dpp<HMIRROR>(t0), o1 = dpp<HMIRROR>(t1);
    const bool lowq = sub < 4;
    const float g_0 = rbf(lowq ? t0 : o0), g_1 = rbf(lowq ? t1 : o1), u_0 = rbf(lowq ? o0 : t0), u_1 = rbf(lowq ? o1 : t1);
    const float act0 = rbf(rbf(gelu_tanh(g_0)) * u_0), act1 = rbf(rbf(gelu_tanh(g_1)) * u_1);
    float a[16], r[2];
#pragma unroll
    for (int e = 0; e < 16; ++e) a[e] = 0.f;
    fma8(a, act0, f2w);
    fma8(a + 8, act1, f2w);
    fold_groups<16>(a, r, bit3);
    *(f32x2*)&L.part[w][(lane >> 5) * 64 + sub * 8 + ((lane >> 4) & 1) * 4 + (bit3 ? 2 : 0)] = f32x2{r[0], r[1]};
    STAMP(21);
    wg_barrier();
    if (tid < 128) {
      float t2 = 0.f;
#pragma unroll
      for (int i = 0; i < 8; ++i) t2 += L.part[i][tid];
      putf(e3b + (size_t)kp2 * (2 * D) + (cb2 * 64 + (tid & 63)) * 2 + (tid >> 6), tag, t2);
    }
  }
  STAMP(9);

  if (has_col) {
    gu64* src = e3b + (size_t)(lane >> 4) * (2 * D) + ccol * 2 + (lane & 15);
    u64 g[6];
    for (unsigned spins = 0; spins < SPIN_CAP; ++spins) {
      bool ok = true;
#pragma unroll
      for (int i = 0; i < 6; ++i) g[i] = ldg(src + (size_t)i * 4 * (2 * D));
#pragma unroll
      for (int i = 0; i < 6; ++i) ok = ok && okf(g[i], tag);
      if (__all(ok)) break;
      __builtin_amdgcn_s_sleep(1);
    }
    STAMP(22);
    float t = ((valf(g[0]) + valf(g[1])) + (valf(g[2]) + valf(g[3]))) + (valf(g[4]) + valf(g[5]));
    t = fold16(t, t);
    t = fold32(t, t);
    if (lane < 16) out[(lane & 1) * D + ccol + (lane >> 1)] = f2bf(hres + rbf(t));
    if (ccol == 0 && lane == 0) __hip_atomic_store((gu64*)(ws + WS_EPOCH), tag, RLX_AGENT);
  }
  STAMP(10);
  if (w == 0 && DIAG_K > 0) {
    const u64 t1 = __builtin_amdgcn_s_memrealtime() + (stv[DIAG_K] - stv[0]);
    while (__builtin_amdgcn_s_memrealtime() < t1) __builtin_amdgcn_s_sleep(1);
  }
}
}

extern "C" void kernel_launch(void* const* d_in, const int* in_sizes, int n_in, void* d_out, int out_size, void* d_ws,
                              size_t ws_size, hipStream_t stream) {
  const unsigned short* x = (const unsigned short*)d_in[0];
  const unsigned short* kc = (const unsigned short*)d_in[1];
  const unsigned short* vc = (const unsigned short*)d_in[2];
  const unsigned short* wq = (const unsigned short*)d_in[3];
  const unsigned short* wo = (const unsigned short*)d_in[4];
  const unsigned short* wg = (const unsigned short*)d_in[6];
  const unsigned short* wu = (const unsigned short*)d_in[7];
  const unsigned short* wd = (const unsigned short*)d_in[8];
  fused_kernel<<<dim3(NWG), dim3(NTHR), 0, stream>>>(x, kc, vc, wq, wo, wg, wu, wd, (unsigned short*)d_out,
                                                      (unsigned char*)d_ws);
}
```

```cpp
#include <hip/hip_runtime.h>
#include <hip/hip_bf16.h>
#include <stdint.h>

#define GAS __attribute__((address_space(1)))
typedef unsigned v4u __attribute__((ext_vector_type(4)));
typedef float f32x4 __attribute__((ext_vector_type(4)));
typedef float f32x2 __attribute__((ext_vector_type(2)));
typedef __bf16 bf16x2 __attribute__((ext_vector_type(2)));
typedef unsigned long long u64;
typedef GAS u64 gu64;
#define RLX_AGENT __ATOMIC_RELAXED, __HIP_MEMORY_SCOPE_AGENT
#define RLX_WG __ATOMIC_RELAXED, __HIP_MEMORY_SCOPE_WORKGROUP

#define POLL_BT 5
#define WB_POS 2
#define L2_WB() do { if (w == 7 && idx == 0) asm volatile("buffer_wbl2 sc1" ::: "memory"); } while (0)
#define L2_WB_ALL() do { if (w == 7) asm volatile("buffer_wbl2 sc1" ::: "memory"); } while (0)
#define ST_NT 0
#define WO_POS 0
#define FFN_POS 0
#define GATE_BT 1
#define SENT_E3A 2
#define SENT_E2B 1
#define SLEEP_N 1
namespace {
constexpr int D = 640, HQ = 32, DH = 192, SK = 8192, WIN = 2048, FF = 1536;
constexpr int QCOLS = HQ * DH;
constexpr int NWG = 256, NTHR = 512;
constexpr int KEYS_WG = WIN / 4;
constexpr unsigned SPIN_CAP = 1u << 20;

constexpr size_t WS_EPOCH = 0;
constexpr size_t WS_E0 = 4096;
constexpr size_t WS_E1 = WS_E0 + (size_t)NWG * 384 * 8;
constexpr size_t WS_E2A = WS_E1 + (size_t)NWG * 256 * 8;
constexpr size_t WS_E2B = WS_E2A + (size_t)D * 64 * 8;
constexpr size_t WS_E3A = WS_E2B + (size_t)8 * D * 8;
constexpr size_t WS_E3B = WS_E3A + (size_t)FF * 16 * 8;

__device__ __forceinline__ float bf2f(unsigned short u) { return __uint_as_float((unsigned)u << 16); }
__device__ __forceinline__ float lo2f(unsigned u) { return __uint_as_float(u << 16); }
__device__ __forceinline__ float hi2f(unsigned u) { return __uint_as_float(u & 0xffff0000u); }
__device__ __forceinline__ unsigned short f2bf(float f) { return __builtin_bit_cast(unsigned short, (__bf16)f); }
__device__ __forceinline__ float rbf(float f) { return (float)(__bf16)f; }
__device__ __forceinline__ float dot2(unsigned k, unsigned q, float acc) {
  return __builtin_amdgcn_fdot2_f32_bf16(__builtin_bit_cast(bf16x2, k), __builtin_bit_cast(bf16x2, q), acc, false);
}
template <int CTRL> __device__ __forceinline__ float dpp(float x) {
  return __int_as_float(__builtin_amdgcn_mov_dpp(__float_as_int(x), CTRL, 0xf, 0xf, true));
}
constexpr int XOR1 = 0xB1, XOR2 = 0x4E, HMIRROR = 0x141, ROR8 = 0x128, QP0 = 0x00, QP2 = 0xAA;
__device__ __forceinline__ float fold32(float a, float b) {
  auto s = __builtin_amdgcn_permlane32_swap(__float_as_uint(a), __float_as_uint(b), false, false);
  return __uint_as_float(s[0]) + __uint_as_float(s[1]);
}
__device__ __forceinline__ float fold16(float a, float b) {
  auto s = __builtin_amdgcn_permlane16_swap(__float_as_uint(a), __float_as_uint(b), false, false);
  return __uint_as_float(s[0]) + __uint_as_float(s[1]);
}
__device__ __forceinline__ float fold8(float a, float b, bool bit3) {
  const float t = a + dpp<ROR8>(a), u = b + dpp<ROR8>(b);
  return bit3 ? u : t;
}
template <int N> __device__ __forceinline__ void fold_groups(const float (&v)[N], float (&r)[N / 8], bool bit3) {
  float a[N / 2], c[N / 4];
#pragma unroll
  for (int i = 0; i < N / 2; ++i) a[i] = fold32(v[i], v[i + N / 2]);
#pragma unroll
  for (int i = 0; i < N / 4; ++i) c[i] = fold16(a[i], a[i + N / 4]);
#pragma unroll
  for (int i = 0; i < N / 8; ++i) r[i] = fold8(c[i], c[i + N / 8], bit3);
}
__device__ __forceinline__ float swap16_max(float x) {
  auto s = __builtin_amdgcn_permlane16_swap(__float_as_uint(x), __float_as_uint(x), false, false);
  return fmaxf(__uint_as_float(s[0]), __uint_as_float(s[1]));
}
__device__ __forceinline__ float swap32_max(float x) {
  auto s = __builtin_amdgcn_permlane32_swap(__float_as_uint(x), __float_as_uint(x), false, false);
  return fmaxf(__uint_as_float(s[0]), __uint_as_float(s[1]));
}
__device__ __forceinline__ float grp_sum(float x) { x += dpp<ROR8>(x); x = fold16(x, x); return fold32(x, x); }
__device__ __forceinline__ float grp_max(float x) { x = fmaxf(x, dpp<ROR8>(x)); x = swap16_max(x); return swap32_max(x); }
__device__ __forceinline__ float oct_sum(float x) { x += dpp<XOR1>(x); x += dpp<XOR2>(x); x += dpp<HMIRROR>(x); return x; }
__device__ __forceinline__ float wave_sum(float x) { return grp_sum(oct_sum(x)); }
__device__ __forceinline__ float half_sum(float x) { x = oct_sum(x); x += dpp<ROR8>(x); return fold16(x, x); }

__device__ __forceinline__ void wg_barrier() {
  asm volatile("s_waitcnt lgkmcnt(0)" ::: "memory");
  __builtin_amdgcn_s_barrier();
  asm volatile("" ::: "memory");
}
__device__ __forceinline__ void lds_wait() { asm volatile("s_waitcnt lgkmcnt(0)" ::: "memory"); }
__device__ __forceinline__ void stg(gu64* g, u64 v) {
  __hip_atomic_store(g, v, RLX_AGENT);
}
__device__ __forceinline__ void putf(gu64* g, u64 tag, float v) { stg(g, (tag << 24) | (u64)(__float_as_uint(v) >> 8)); }
__device__ __forceinline__ u64 ldg(gu64* g) { return __hip_atomic_load(g, RLX_AGENT); }
__device__ __forceinline__ bool okf(u64 x, u64 tag) { return (x >> 24) == tag; }
__device__ __forceinline__ float valf(u64 x) { return __uint_as_float(((unsigned)x) << 8); }
__device__ __forceinline__ void putu(gu64* g, u64 tag, unsigned v) { stg(g, (tag << 32) | (u64)v); }
__device__ __forceinline__ bool oku(u64 x, u64 tag) { return (unsigned)(x >> 32) == (unsigned)tag; }
__device__ __forceinline__ void wait_f(gu64* g, u64 tag) {
  for (unsigned spins = 0; spins < SPIN_CAP; ++spins) {
    if (okf(ldg(g), tag)) break;
    __builtin_amdgcn_s_sleep(2);
  }
}
__device__ __forceinline__ v4u ldnt(const void* p) { return __builtin_nontemporal_load((const GAS v4u*)p); }

__device__ __forceinline__ void ln_stats(const float (&v)[10], float& mean_bf, float& rsd) {
  float s = 0.f;
#pragma unroll
  for (int i = 0; i < 10; ++i) s += v[i];
  const float mean = wave_sum(s) * (1.0f / D);
  float q = 0.f;
#pragma unroll
  for (int i = 0; i < 10; ++i) { const float d = v[i] - mean; q = fmaf(d, d, q); }
  const float var = wave_sum(q) * (1.0f / D);
  mean_bf = rbf(mean);
  rsd = __builtin_amdgcn_rcpf(rbf(__builtin_amdgcn_sqrtf(rbf(rbf(var) + 1e-5f))));
}
__device__ __forceinline__ float ln_apply(float x, float mean_bf, float rsd) { return rbf(rbf(x - mean_bf) * rsd); }
__device__ __forceinline__ void fma8(float* a, float s, v4u t) {
#pragma unroll
  for (int e = 0; e < 4; ++e) { a[2 * e] = fmaf(s, lo2f(t[e]), a[2 * e]); a[2 * e + 1] = fmaf(s, hi2f(t[e]), a[2 * e + 1]); }
}
__device__ __forceinline__ float gelu_tanh(float g) {
  const float y = 0.7978845608028654f * (g + 0.044715f * g * g * g);
  const float t = 1.0f - 2.0f * __builtin_amdgcn_rcpf(__expf(2.0f * y) + 1.0f);
  return 0.5f * g * (1.0f + t);
}

#define STAMP(i) do { } while (0)

struct Lds {
  float xn[5][2][80];
  float part[48][160];
  float raw[8][200];
  float on[2][DH];
  float hv[2][D];
  float mw[8], lw[8];
  float red[8][128];
  unsigned short qs[DH];
  unsigned pcnt, qcnt, qflag;
};

__global__ void __launch_bounds__(NTHR, 2)
fused_kernel(const unsigned short* __restrict__ x, const unsigned short* __restrict__ kc, const unsigned short* __restrict__ vc,
             const unsigned short* __restrict__ wq, const unsigned short* __restrict__ wo, const unsigned short* __restrict__ wg,
             const unsigned short* __restrict__ wu, const unsigned short* __restrict__ wd, unsigned short* __restrict__ out,
             unsigned char* ws) {
  __shared__ __attribute__((aligned(16))) Lds L;
  const int tid = threadIdx.x, lane = tid & 63, w = __builtin_amdgcn_readfirstlane(tid >> 6);
  const int bid = blockIdx.x, xcd = bid & 7, idx = bid >> 3;
  const int h = xcd * 4 + (idx >> 3), j = idx & 7, b = j >> 2, s = j & 3;
  const int grp = lane >> 3, sub = lane & 7;
  const bool bit3 = (lane & 8) != 0;
  const int peer0 = xcd | ((idx & ~7) << 3);
  const bool is_f1 = bid < 192;
  const int cb1 = bid % 24, m1 = (bid / 24) & 1, kq1 = bid / 48;
  const bool is_f2 = bid < 240;
  const int cb2 = bid % 10, kp2 = bid / 10;
  const bool has_col = bid >= 176 && w == 0;
  const int ccol = (bid >= 176 ? bid - 176 : 0) * 8;

  gu64* e0 = (gu64*)(ws + WS_E0);
  gu64* e1 = (gu64*)(ws + WS_E1);
  gu64* e2a = (gu64*)(ws + WS_E2A);
  gu64* e2b = (gu64*)(ws + WS_E2B);
  gu64* e3a = (gu64*)(ws + WS_E3A);
  gu64* e3b = (gu64*)(ws + WS_E3B);

  u64 stv[32];
#pragma unroll
  for (int i = 0; i < 32; ++i) stv[i] = 0;
  STAMP(0);
  if (WB_POS == 1) L2_WB();
  if (WB_POS == 3) L2_WB_ALL();
  const u64 tag = (ldg((gu64*)(ws + WS_EPOCH)) + 1ull) & 0xFFFFFFFFFFull;
  const unsigned short xres_raw = x[(lane & 1) * D + ccol + ((lane >> 1) & 7)];
  unsigned xr[2][5];
  v4u wqv[6];
  if (w < 5) {
#pragma unroll
    for (int bb = 0; bb < 2; ++bb)
#pragma unroll
      for (int i = 0; i < 5; ++i) xr[bb][i] = *(const GAS unsigned*)(x + bb * D + 2 * (lane + 64 * i));
    const unsigned short* p = wq + (size_t)(j * 80 + w * 16 + grp) * QCOLS + h * DH + sub * 8;
#pragma unroll
    for (int g = 0; g < 2; ++g)
#pragma unroll
      for (int c = 0; c < 3; ++c) wqv[g * 3 + c] = ldnt(p + (size_t)g * 8 * QCOLS + c * 64);
  }
  if (tid == 0) { L.pcnt = 0u; L.qcnt = 0u; L.qflag = 0u; }
  __builtin_amdgcn_sched_barrier(0);
  wg_barrier();
  const size_t kvoff = ((size_t)(b * HQ + h) * SK + (SK - WIN) + s * KEYS_WG + w * 64 + grp) * DH + sub * 8;
  v4u kv[24], vv[24];
  {
    const bool poller = w >= 1 && w <= 3;
    const int p0 = (w - 1) * 3, np = w == 3 ? 2 : 3;
    gu64* src = e0 + (size_t)(peer0 + 8 * (poller ? p0 : 0)) * 384 + b * DH + lane;
    u64 g[3][3];
#pragma unroll
    for (int bt = 0; bt < 8; ++bt) {
      if (bt == GATE_BT) { while (__builtin_amdgcn_readfirstlane(__hip_atomic_load(&L.pcnt, RLX_WG)) < 100u) __builtin_amdgcn_s_sleep(1); }
      if (bt == POLL_BT && poller) {
#pragma unroll
        for (int i = 0; i < 3; ++i)
#pragma unroll
          for (int k = 0; k < 3; ++k) g[i][k] = ldg(src + (size_t)(i < np ? i : 0) * 8 * 384 + 64 * k);
      }
      __builtin_amdgcn_sched_barrier(0);
#pragma unroll
      for (int gg = 0; gg < 2; ++gg)
#pragma unroll
        for (int c = 0; c < 3; ++c) {
          if (bt < 4) kv[(bt * 2 + gg) * 3 + c] = ldnt(kc + kvoff + (bt * 2 + gg) * 8 * DH + c * 64);
          else vv[((bt - 4) * 2 + gg) * 3 + c] = ldnt(vc + kvoff + ((bt - 4) * 2 + gg) * 8 * DH + c * 64);
        }
      __builtin_amdgcn_sched_barrier(0);
      if (bt == GATE_BT - 1 && w < 5) {
#pragma unroll
        for (int bb = 0; bb < 2; ++bb) {
          float xv[10];
#pragma unroll
          for (int i = 0; i < 5; ++i) { xv[2 * i] = lo2f(xr[bb][i]); xv[2 * i + 1] = hi2f(xr[bb][i]); }
          float mb, rs;
          ln_stats(xv, mb, rs);
#pragma unroll
          for (int i = 0; i < 5; ++i) {
            const int e = 2 * (lane + 64 * i) - j * 80;
            if (e >= 0 && e < 80) *(f32x2*)&L.xn[w][bb][e] = f32x2{ln_apply(xv[2 * i], mb, rs), ln_apply(xv[2 * i + 1], mb, rs)};
          }
        }
        lds_wait();
        float qa[48], qr[6];
#pragma unroll
        for (int i = 0; i < 48; ++i) qa[i] = 0.f;
#pragma unroll
        for (int g = 0; g < 2; ++g) {
          const float x0 = L.xn[w][0][w * 16 + g * 8 + grp], x1 = L.xn[w][1][w * 16 + g * 8 + grp];
#pragma unroll
          for (int c = 0; c < 3; ++c) { fma8(qa + c * 8, x0, wqv[g * 3 + c]); fma8(qa + 24 + c * 8, x1, wqv[g * 3 + c]); }
        }
        fold_groups<48>(qa, qr, bit3);
        {
          const int i0 = ((lane >> 4) & 1) * 12 + (bit3 ? 6 : 0);
          float* dst = &L.part[0][0] + w * 384 + (lane >> 5) * DH + sub * 8;
#pragma unroll
          for (int k = 0; k < 6; ++k) dst[((i0 + k) >> 3) * 64 + ((i0 + k) & 7)] = qr[k];
        }
        lds_wait();
        unsigned old = 0;
        if (lane == 0) old = __hip_atomic_fetch_add(&L.pcnt, 1u, RLX_WG);
        if (__builtin_amdgcn_readfirstlane(old) == 4u) {
#pragma unroll
          for (int k = 0; k < 6; ++k) {
            const float* sp = &L.part[0][0] + lane + 64 * k;
            putf(e0 + bid * 384 + lane + 64 * k, tag, (sp[0] + sp[384]) + (sp[768] + sp[1152]) + sp[1536]);
          }
          __builtin_amdgcn_sched_barrier(0);
          if (lane == 0) __hip_atomic_store(&L.pcnt, 100u, RLX_WG);
        }
        STAMP(1);
      }
    }
    if (poller) {
      bool ok = true;
#pragma unroll
      for (int i = 0; i < 3; ++i)
#pragma unroll
        for (int k = 0; k < 3; ++k) ok = ok && okf(g[i][k], tag);
      if (__all(ok)) {
#pragma unroll
        for (int i = 0; i < 3; ++i)
          if (i < np) {
#pragma unroll
            for (int k = 0; k < 3; ++k) L.raw[p0 + i][64 * k + lane] = valf(g[i][k]);
          }
      } else {
        for (int i = 0; i < np; ++i) {
          gu64* sp = src + (size_t)i * 8 * 384;
          wait_f(sp - lane, tag);
          for (unsigned spins = 0; spins < SPIN_CAP; ++spins) {
            const u64 r0 = ldg(sp), r1 = ldg(sp + 64), r2 = ldg(sp + 128);
            L.raw[p0 + i][lane] = valf(r0); L.raw[p0 + i][64 + lane] = valf(r1); L.raw[p0 + i][128 + lane] = valf(r2);
            if (__all(okf(r0, tag) && okf(r1, tag) && okf(r2, tag))) break;
            __builtin_amdgcn_s_sleep(2);
          }
        }
      }
      lds_wait();
      unsigned old = 0;
      if (lane == 0) old = __hip_atomic_fetch_add(&L.qcnt, 1u, RLX_WG);
      if (__builtin_amdgcn_readfirstlane(old) == 2u) {
#pragma unroll
        for (int k = 0; k < 3; ++k) {
          float q = 0.f;
#pragma unroll
          for (int i = 0; i < 8; ++i) q += L.raw[i][lane + 64 * k];
          L.qs[lane + 64 * k] = f2bf(q);
        }
        lds_wait();
        if (lane == 0) __hip_atomic_store(&L.qflag, 1u, RLX_WG);
      }
    }
    while (__builtin_amdgcn_readfirstlane(__hip_atomic_load(&L.qflag, RLX_WG)) == 0u) __builtin_amdgcn_s_sleep(1);
    asm volatile("" ::: "memory");
  }
  STAMP(2);
  v4u qp[3];
#pragma unroll
  for (int c = 0; c < 3; ++c) qp[c] = *(const v4u*)&L.qs[c * 64 + sub * 8];

  float sc[8];
  {
#pragma unroll
    for (int g = 0; g < 8; ++g) {
      float a = 0.f;
#pragma unroll
      for (int c = 0; c < 3; ++c)
#pragma unroll
        for (int e = 0; e < 4; ++e) a = dot2(kv[g * 3 + c][e], qp[c][e], a);
      a = oct_sum(a);
      sc[g] = rbf(rbf(a) * 0.07216878364870322f);
    }
    float m = sc[0];
#pragma unroll
    for (int g = 1; g < 8; ++g) m = fmaxf(m, sc[g]);
    m = grp_max(m);
    if (lane == 0) L.mw[w] = m;
  }
  STAMP(3);

  v4u wov[4], f1w[3], f2w;
  {
    const int r10 = lane / 10, c10 = lane - r10 * 10;
    const unsigned short* p = wo + (size_t)(h * DH + w * 24 + (lane < 60 ? r10 : 0)) * D + j * 80 + (lane < 60 ? c10 : 0) * 8;
#pragma unroll
    for (int i = 0; i < 4; ++i) wov[i] = ldnt(p + (size_t)i * 6 * D);
  }
  __builtin_amdgcn_sched_barrier(0);
  {
    const unsigned short* p1 = (m1 ? wu : wg) + (size_t)((is_f1 ? kq1 : 0) * 160 + (tid >> 3)) * FF + cb1 * 64 + sub * 8;
    f1w[0] = ldnt(p1);
    f1w[1] = ldnt(p1 + (size_t)64 * FF);
    f1w[2] = ldnt(p1 + (size_t)(tid < 256 ? 128 : 0) * FF);
    f2w = ldnt(wd + (size_t)((is_f2 ? kp2 : 0) * 64 + (tid >> 3)) * D + cb2 * 64 + sub * 8);
  }
  __builtin_amdgcn_sched_barrier(0);
  __builtin_amdgcn_sched_barrier(0);
  wg_barrier();

  float M;
  {
    const f32x4 ma = *(const f32x4*)&L.mw[0], mb = *(const f32x4*)&L.mw[4];
    M = fmaxf(fmaxf(fmaxf(ma[0], ma[1]), fmaxf(ma[2], ma[3])), fmaxf(fmaxf(mb[0], mb[1]), fmaxf(mb[2], mb[3])));
    float l = 0.f, p[8];
#pragma unroll
    for (int g = 0; g < 8; ++g) { p[g] = __expf(sc[g] - M); l += p[g]; }
    l = grp_sum(l);
    if (lane == 0) L.lw[w] = l;
    float acc[24], r[3];
#pragma unroll
    for (int i = 0; i < 24; ++i) acc[i] = 0.f;
#pragma unroll
    for (int g = 0; g < 8; ++g)
#pragma unroll
      for (int c = 0; c < 3; ++c) fma8(acc + c * 8, p[g], vv[g * 3 + c]);
    fold_groups<24>(acc, r, bit3);
    const int i0 = (lane >> 5) * 12 + ((lane >> 4) & 1) * 6 + (bit3 ? 3 : 0);
#pragma unroll
    for (int k = 0; k < 3; ++k) L.raw[w][((i0 + k) >> 3) * 64 + sub * 8 + ((i0 + k) & 7)] = r[k];
  }
  wg_barrier();
  if (tid < 194) {
    float o = 0.f;
#pragma unroll
    for (int i = 0; i < 8; ++i) o += tid < DH ? L.raw[i][tid] : L.lw[i];
    putf(e1 + bid * 256 + tid, tag, tid == DH ? M : o);
  }
  wg_barrier();
  STAMP(4);
  if (WB_POS == 2) L2_WB();
  if (WB_POS == 4) L2_WB_ALL();

  {
    gu64* src = e1 + (size_t)(peer0 + 8 * w) * 256 + lane;
    const int o3 = lane < 2 ? 192 : 0;
    u64 g0 = ldg(src), g1 = ldg(src + 64), g2 = ldg(src + 128), g3 = ldg(src + o3);
    if (!__all(okf(g0, tag) && okf(g1, tag) && okf(g2, tag) && okf(g3, tag))) {
      wait_f(src - lane + 193, tag);
      STAMP(11);
      for (unsigned spins = 0; spins < SPIN_CAP; ++spins) {
        g0 = ldg(src); g1 = ldg(src + 64); g2 = ldg(src + 128); g3 = ldg(src + o3);
        if (__all(okf(g0, tag) && okf(g1, tag) && okf(g2, tag) && okf(g3, tag))) break;
        __builtin_amdgcn_s_sleep(1);
      }
    }
    STAMP(12);
    L.raw[w][lane] = valf(g0); L.raw[w][64 + lane] = valf(g1); L.raw[w][128 + lane] = valf(g2);
    if (lane < 2) L.raw[w][192 + lane] = valf(g3);
  }
  wg_barrier();
  STAMP(5);
  if (tid < 2 * DH) {
    const int bb = tid >= DH ? 1 : 0, d = tid - bb * DH;
    const float m0 = L.raw[bb * 4][192], m1v = L.raw[bb * 4 + 1][192], m2 = L.raw[bb * 4 + 2][192], m3 = L.raw[bb * 4 + 3][192];
    const float MM = fmaxf(fmaxf(m0, m1v), fmaxf(m2, m3));
    const float w0 = __expf(m0 - MM), w1 = __expf(m1v - MM), w2 = __expf(m2 - MM), w3 = __expf(m3 - MM);
    const float o = (w0 * L.raw[bb * 4][d] + w1 * L.raw[bb * 4 + 1][d]) + (w2 * L.raw[bb * 4 + 2][d] + w3 * L.raw[bb * 4 + 3][d]);
    const float l = (w0 * L.raw[bb * 4][193] + w1 * L.raw[bb * 4 + 1][193]) + (w2 * L.raw[bb * 4 + 2][193] + w3 * L.raw[bb * 4 + 3][193]);
    L.on[bb][d] = rbf(o * __builtin_amdgcn_rcpf(l));
  }
  wg_barrier();
  if (lane < 60) {
    const int r10 = lane / 10, c10 = lane - r10 * 10;
    float a0[8], a1[8];
#pragma unroll
    for (int e = 0; e < 8; ++e) { a0[e] = 0.f; a1[e] = 0.f; }
#pragma unroll
    for (int i = 0; i < 4; ++i) {
      const int row = w * 24 + i * 6 + r10;
      fma8(a0, L.on[0][row], wov[i]);
      fma8(a1, L.on[1][row], wov[i]);
    }
    float* dst = &L.part[w * 6 + r10][c10 * 16];
#pragma unroll
    for (int e = 0; e < 8; e += 2) *(f32x4*)(dst + 2 * e) = f32x4{a0[e], a1[e], a0[e + 1], a1[e + 1]};
  }
  wg_barrier();
  if (tid < 320) {
    const int o = tid >> 1, hf = (tid & 1) * 24;
    float t0 = 0.f, t1 = 0.f, t2 = 0.f, t3 = 0.f;
#pragma unroll
    for (int i = 0; i < 24; i += 4) { t0 += L.part[hf + i][o]; t1 += L.part[hf + i + 1][o]; t2 += L.part[hf + i + 2][o]; t3 += L.part[hf + i + 3][o]; }
    float t = (t0 + t1) + (t2 + t3);
    t += dpp<XOR1>(t);
    if ((tid & 1) == 0) putf(e2a + (size_t)h * (2 * D) + j * 160 + o, tag, t);
  }
  STAMP(6);

  float hres = 0.f;
  if (has_col) {
    gu64* src = e2a + (size_t)(lane >> 4) * (2 * D) + ccol * 2 + (lane & 15);
    u64 g[8];
    for (unsigned spins = 0; spins < SPIN_CAP; ++spins) {
      bool ok = true;
#pragma unroll
      for (int i = 0; i < 8; ++i) g[i] = ldg(src + (size_t)i * 4 * (2 * D));
#pragma unroll
      for (int i = 0; i < 8; ++i) ok = ok && okf(g[i], tag);
      if (__all(ok)) break;
      __builtin_amdgcn_s_sleep(1);
    }
    STAMP(13);
    float t = ((valf(g[0]) + valf(g[1])) + (valf(g[2]) + valf(g[3]))) + ((valf(g[4]) + valf(g[5])) + (valf(g[6]) + valf(g[7])));
    t = fold16(t, t);
    t = fold32(t, t);
    hres = rbf(bf2f(xres_raw) + rbf(t));
    const unsigned hb = f2bf(hres);
    const unsigned other = __builtin_amdgcn_mov_dpp(hb, XOR1, 0xf, 0xf, true);
    if ((lane & 1) == 0) {
      putu(e2b + (lane >> 4) * D + ccol + ((lane >> 1) & 7), tag, hb | (other << 16));
      putu(e2b + ((lane >> 4) + 4) * D + ccol + ((lane >> 1) & 7), tag, hb | (other << 16));
    }
    STAMP(14);
  }
  wg_barrier();
  STAMP(7);

  if (is_f1) {
    {
      const int o1 = tid < 128 ? 512 : 0;
      gu64* src = e2b + xcd * D + tid;
      if (SENT_E2B == 1) { for (unsigned spins = 0; spins < SPIN_CAP; ++spins) { if (oku(ldg(src - lane), tag)) break; __builtin_amdgcn_s_sleep(2); } }
      STAMP(15);
      u64 g0 = ldg(src), g1 = ldg(src + o1);
      for (unsigned spins = 0; !__all(oku(g0, tag) && oku(g1, tag)) && spins < SPIN_CAP; ++spins) {
        __builtin_amdgcn_s_sleep(SLEEP_N);
        g0 = ldg(src); g1 = ldg(src + o1);
      }
      STAMP(16);
      L.hv[0][tid] = lo2f((unsigned)g0); L.hv[1][tid] = hi2f((unsigned)g0);
      if (tid < 128) { L.hv[0][512 + tid] = lo2f((unsigned)g1); L.hv[1][512 + tid] = hi2f((unsigned)g1); }
    }
    wg_barrier();
    {
      STAMP(17);
      float m0, s0, m1v, s1;
      const int r0 = kq1 * 160 + (tid >> 3);
      float hr0[3], hr1[3];
      {
        f32x4 v[5];
#pragma unroll
        for (int i = 0; i < 5; ++i) v[i] = *(const f32x4*)&L.hv[lane >> 5][(lane & 31) * 4 + 128 * i];
#pragma unroll
        for (int i = 0; i < 3; ++i) { const int row = (i < 2 || tid < 256) ? r0 + 64 * i : 0; hr0[i] = L.hv[0][row]; hr1[i] = L.hv[1][row]; }
        const f32x4 sv = (v[0] + v[1]) + (v[2] + v[3]) + v[4];
        const float mean = half_sum((sv[0] + sv[1]) + (sv[2] + sv[3])) * (1.0f / D);
        f32x4 qv = {0.f, 0.f, 0.f, 0.f};
#pragma unroll
        for (int i = 0; i < 5; ++i) { const f32x4 d = v[i] - mean; qv += d * d; }
        const float var = half_sum((qv[0] + qv[1]) + (qv[2] + qv[3])) * (1.0f / D);
        const float mb = rbf(mean), rs = __builtin_amdgcn_rcpf(rbf(__builtin_amdgcn_sqrtf(rbf(rbf(var) + 1e-5f))));
        auto pm = __builtin_amdgcn_permlane32_swap(__float_as_uint(mb), __float_as_uint(mb), false, false);
        auto pr = __builtin_amdgcn_permlane32_swap(__float_as_uint(rs), __float_as_uint(rs), false, false);
        m0 = __uint_as_float(pm[0]); m1v = __uint_as_float(pm[1]); s0 = __uint_as_float(pr[0]); s1 = __uint_as_float(pr[1]);
      }
      float a[16], r[2];
#pragma unroll
      for (int e = 0; e < 16; ++e) a[e] = 0.f;
#pragma unroll
      for (int i = 0; i < 3; ++i) {
        const bool live = i < 2 || tid < 256;
        fma8(a, live ? ln_apply(hr0[i], m0, s0) : 0.f, f1w[i]);
        fma8(a + 8, live ? ln_apply(hr1[i], m1v, s1) : 0.f, f1w[i]);
      }
      fold_groups<16>(a, r, bit3);
      *(f32x2*)&L.red[w][(lane >> 5) * 64 + sub * 8 + ((lane >> 4) & 1) * 4 + (bit3 ? 2 : 0)] = f32x2{r[0], r[1]};
      STAMP(18);
    }
    wg_barrier();
    if (tid < 128) {
      float t = 0.f;
#pragma unroll
      for (int i = 0; i < 8; ++i) t += L.red[i][tid];
      putf(e3a + (size_t)((cb1 * 2 + m1) * 4 + kq1) * 128 + (tid & 63) * 2 + (tid >> 6), tag, t);
    }
  }
  STAMP(8);

  if (is_f2) {
    gu64* src = e3a + (size_t)(kp2 * 8 + sub) * 128 + (tid >> 3) * 2;
    if (SENT_E3A == 1) {
      gu64* sent = e3a + (size_t)(kp2 * 8 + sub) * 128 + w * 16;
      for (unsigned spins = 0; spins < SPIN_CAP; ++spins) { if (__all(okf(ldg(sent), tag))) break; __builtin_amdgcn_s_sleep(2); }
    } else if (SENT_E3A == 2) {
      if (w == 0) {
        gu64* sent = e3a + (size_t)(kp2 * 8 + sub) * 128 + (grp & 1) * 127;
        for (unsigned spins = 0; spins < SPIN_CAP; ++spins) { if (__all(okf(ldg(sent), tag))) break; __builtin_amdgcn_s_sleep(1); }
      }
      wg_barrier();
    }
    STAMP(19);
    u64 g0 = ldg(src), g1 = ldg(src + 1);
    for (unsigned spins = 0; !__all(okf(g0, tag) && okf(g1, tag)) && spins < SPIN_CAP; ++spins) {
      __builtin_amdgcn_s_sleep(SLEEP_N);
      g0 = ldg(src); g1 = ldg(src + 1);
    }
    STAMP(20);
    float t0 = valf(g0), t1 = valf(g1);
    t0 += dpp<XOR1>(t0); t1 += dpp<XOR1>(t1);
    t0 += dpp<XOR2>(t0); t1 += dpp<XOR2>(t1);
    const float o0 = dpp<HMIRROR>(t0), o1 = dpp<HMIRROR>(t1);
    const bool lowq = sub < 4;
    const float g_0 = rbf(lowq ? t0 : o0), g_1 = rbf(lowq ? t1 : o1), u_0 = rbf(lowq ? o0 : t0), u_1 = rbf(lowq ? o1 : t1);
    const float act0 = rbf(rbf(gelu_tanh(g_0)) * u_0), act1 = rbf(rbf(gelu_tanh(g_1)) * u_1);
    float a[16], r[2];
#pragma unroll
    for (int e = 0; e < 16; ++e) a[e] = 0.f;
    fma8(a, act0, f2w);
    fma8(a + 8, act1, f2w);
    fold_groups<16>(a, r, bit3);
    *(f32x2*)&L.part[w][(lane >> 5) * 64 + sub * 8 + ((lane >> 4) & 1) * 4 + (bit3 ? 2 : 0)] = f32x2{r[0], r[1]};
    STAMP(21);
    wg_barrier();
    if (tid < 128) {
      float t2 = 0.f;
#pragma unroll
      for (int i = 0; i < 8; ++i) t2 += L.part[i][tid];
      putf(e3b + (size_t)kp2 * (2 * D) + (cb2 * 64 + (tid & 63)) * 2 + (tid >> 6), tag, t2);
    }
  }
  STAMP(9);

  if (has_col) {
    gu64* src = e3b + (size_t)(lane >> 4) * (2 * D) + ccol * 2 + (lane & 15);
    u64 g[6];
    for (unsigned spins = 0; spins < SPIN_CAP; ++spins) {
      bool ok = true;
#pragma unroll
      for (int i = 0; i < 6; ++i) g[i] = ldg(src + (size_t)i * 4 * (2 * D));
#pragma unroll
      for (int i = 0; i < 6; ++i) ok = ok && okf(g[i], tag);
      if (__all(ok)) break;
      __builtin_amdgcn_s_sleep(1);
    }
    STAMP(22);
    float t = ((valf(g[0]) + valf(g[1])) + (valf(g[2]) + valf(g[3]))) + (valf(g[4]) + valf(g[5]));
    t = fold16(t, t);
    t = fold32(t, t);
    if (lane < 16) out[(lane & 1) * D + ccol + (lane >> 1)] = f2bf(hres + rbf(t));
    if (ccol == 0 && lane == 0) __hip_atomic_store((gu64*)(ws + WS_EPOCH), tag, RLX_AGENT);
  }
  STAMP(10);
}
}

extern "C" void kernel_launch(void* const* d_in, const int* in_sizes, int n_in, void* d_out, int out_size, void* d_ws,
                              size_t ws_size, hipStream_t stream) {
  const unsigned short* x = (const unsigned short*)d_in[0];
  const unsigned short* kc = (const unsigned short*)d_in[1];
  const unsigned short* vc = (const unsigned short*)d_in[2];
  const unsigned short* wq = (const unsigned short*)d_in[3];
  const unsigned short* wo = (const unsigned short*)d_in[4];
  const unsigned short* wg = (const unsigned short*)d_in[6];
  const unsigned short* wu = (const unsigned short*)d_in[7];
  const unsigned short* wd = (const unsigned short*)d_in[8];
  fused_kernel<<<dim3(NWG), dim3(NTHR), 0, stream>>>(x, kc, vc, wq, wo, wg, wu, wd, (unsigned short*)d_out,
                                                      (unsigned char*)d_ws);
}
```

```cpp
#define DIAG 0
#include <hip/hip_runtime.h>
#include <hip/hip_bf16.h>
#include <stdint.h>

#define GAS __attribute__((address_space(1)))
typedef unsigned v4u __attribute__((ext_vector_type(4)));
typedef float f32x4 __attribute__((ext_vector_type(4)));
typedef float f32x2 __attribute__((ext_vector_type(2)));
typedef __bf16 bf16x2 __attribute__((ext_vector_type(2)));
typedef unsigned long long u64;
typedef GAS u64 gu64;
#define RLX_AGENT __ATOMIC_RELAXED, __HIP_MEMORY_SCOPE_AGENT

namespace {
constexpr int D = 640, HQ = 32, DH = 192, SK = 8192, WIN = 2048, FF = 1536;
constexpr int QCOLS = HQ * DH;
constexpr int NWG = 256, NTHR = 512;
constexpr int KEYS_WG = WIN / 4;
constexpr unsigned SPIN_CAP = 1u << 20;

constexpr size_t WS_EPOCH = 0;
constexpr size_t WS_E0 = 4096;
constexpr size_t WS_E1 = WS_E0 + (size_t)NWG * 384 * 8;
constexpr size_t WS_E2A = WS_E1 + (size_t)NWG * 256 * 8;
constexpr size_t WS_E2B = WS_E2A + (size_t)D * 64 * 8;
constexpr size_t WS_E3A = WS_E2B + (size_t)D * 8;
constexpr size_t WS_E3B = WS_E3A + (size_t)FF * 16 * 8;

__device__ __forceinline__ float bf2f(unsigned short u) { return __uint_as_float((unsigned)u << 16); }
__device__ __forceinline__ float lo2f(unsigned u) { return __uint_as_float(u << 16); }
__device__ __forceinline__ float hi2f(unsigned u) { return __uint_as_float(u & 0xffff0000u); }
__device__ __forceinline__ unsigned short f2bf(float f) { return __builtin_bit_cast(unsigned short, (__bf16)f); }
__device__ __forceinline__ float rbf(float f) { return (float)(__bf16)f; }
__device__ __forceinline__ float dot2(unsigned k, unsigned q, float acc) {
  return __builtin_amdgcn_fdot2_f32_bf16(__builtin_bit_cast(bf16x2, k), __builtin_bit_cast(bf16x2, q), acc, false);
}
template <int CTRL> __device__ __forceinline__ float dpp(float x) {
  return __int_as_float(__builtin_amdgcn_mov_dpp(__float_as_int(x), CTRL, 0xf, 0xf, true));
}
constexpr int XOR1 = 0xB1, XOR2 = 0x4E, HMIRROR = 0x141, ROR8 = 0x128, QP0 = 0x00, QP2 = 0xAA;
__device__ __forceinline__ float swap16_sum(float x) {
  auto s = __builtin_amdgcn_permlane16_swap(__float_as_uint(x), __float_as_uint(x), false, false);
  return __uint_as_float(s[0]) + __uint_as_float(s[1]);
}
__device__ __forceinline__ float swap32_sum(float x) {
  auto s = __builtin_amdgcn_permlane32_swap(__float_as_uint(x), __float_as_uint(x), false, false);
  return __uint_as_float(s[0]) + __uint_as_float(s[1]);
}
__device__ __forceinline__ float swap16_max(float x) {
  auto s = __builtin_amdgcn_permlane16_swap(__float_as_uint(x), __float_as_uint(x), false, false);
  return fmaxf(__uint_as_float(s[0]), __uint_as_float(s[1]));
}
__device__ __forceinline__ float swap32_max(float x) {
  auto s = __builtin_amdgcn_permlane32_swap(__float_as_uint(x), __float_as_uint(x), false, false);
  return fmaxf(__uint_as_float(s[0]), __uint_as_float(s[1]));
}
__device__ __forceinline__ float grp_sum(float x) { x += dpp<ROR8>(x); x = swap16_sum(x); return swap32_sum(x); }
__device__ __forceinline__ float grp_max(float x) { x = fmaxf(x, dpp<ROR8>(x)); x = swap16_max(x); return swap32_max(x); }
__device__ __forceinline__ float oct_sum(float x) { x += dpp<XOR1>(x); x += dpp<XOR2>(x); x += dpp<HMIRROR>(x); return x; }
__device__ __forceinline__ float wave_sum(float x) { return grp_sum(oct_sum(x)); }
__device__ __forceinline__ float half_sum(float x) { x = oct_sum(x); x += dpp<ROR8>(x); return swap16_sum(x); }

__device__ __forceinline__ void wg_barrier() {
  asm volatile("s_waitcnt lgkmcnt(0)" ::: "memory");
  __builtin_amdgcn_s_barrier();
  asm volatile("" ::: "memory");
}
__device__ __forceinline__ void putf(gu64* g, u64 tag, float v) {
  __hip_atomic_store(g, (tag << 24) | (u64)(__float_as_uint(v) >> 8), RLX_AGENT);
}
__device__ __forceinline__ u64 ldg(gu64* g) { return __hip_atomic_load(g, RLX_AGENT); }
__device__ __forceinline__ bool okf(u64 x, u64 tag) { return (x >> 24) == tag; }
__device__ __forceinline__ float valf(u64 x) { return __uint_as_float(((unsigned)x) << 8); }
__device__ __forceinline__ void putu(gu64* g, u64 tag, unsigned v) { __hip_atomic_store(g, (tag << 32) | (u64)v, RLX_AGENT); }
__device__ __forceinline__ bool oku(u64 x, u64 tag) { return (unsigned)(x >> 32) == (unsigned)tag; }

__device__ __forceinline__ v4u ldnt(const void* p) { return __builtin_nontemporal_load((const GAS v4u*)p); }
__device__ __forceinline__ v4u ld16(const void* p) { return *(const GAS v4u*)p; }

__device__ __forceinline__ void ln_stats(const float (&v)[10], float& mean_bf, float& sd_bf) {
  float s = 0.f;
#pragma unroll
  for (int i = 0; i < 10; ++i) s += v[i];
  const float mean = wave_sum(s) * (1.0f / D);
  float q = 0.f;
#pragma unroll
  for (int i = 0; i < 10; ++i) { const float d = v[i] - mean; q = fmaf(d, d, q); }
  const float var = wave_sum(q) * (1.0f / D);
  mean_bf = rbf(mean);
  sd_bf = rbf(sqrtf(rbf(rbf(var) + 1e-5f)));
}
__device__ __forceinline__ float ln_apply(float x, float mean_bf, float sd_bf) { return rbf(rbf(x - mean_bf) / sd_bf); }
__device__ __forceinline__ void fma8(float (&a)[8], float s, v4u t) {
#pragma unroll
  for (int e = 0; e < 4; ++e) { a[2 * e] = fmaf(s, lo2f(t[e]), a[2 * e]); a[2 * e + 1] = fmaf(s, hi2f(t[e]), a[2 * e + 1]); }
}

#define STAMP(i) do { } while (0)

struct Lds {
  float xn[5][2][80];
  float part[48][160];
  float raw[8][200];
  float on[2][DH];
  float hv[2][D];
  float mw[8], lw[8];
  float red[8][128];
  unsigned short qs[DH];
};

__global__ void __launch_bounds__(NTHR, 2)
fused_kernel(const unsigned short* __restrict__ x, const unsigned short* __restrict__ kc, const unsigned short* __restrict__ vc,
             const unsigned short* __restrict__ wq, const unsigned short* __restrict__ wo, const unsigned short* __restrict__ wg,
             const unsigned short* __restrict__ wu, const unsigned short* __restrict__ wd, unsigned short* __restrict__ out,
             unsigned char* ws) {
  __shared__ __attribute__((aligned(16))) Lds L;
  const int tid = threadIdx.x, lane = tid & 63, w = __builtin_amdgcn_readfirstlane(tid >> 6);
  const int bid = blockIdx.x, xcd = bid & 7, idx = bid >> 3;
  const int h = xcd * 4 + (idx >> 3), j = idx & 7, b = j >> 2, s = j & 3;
  const int grp = lane >> 3, sub = lane & 7;
  const int peer0 = xcd | ((idx & ~7) << 3);
  const bool is_f1 = bid < 192;
  const int cb1 = bid % 24, m1 = (bid / 24) & 1, kq1 = bid / 48;
  const bool is_f2 = bid < 240;
  const int cb2 = bid % 10, kp2 = bid / 10;
  const int ccol = bid + 256 * w;
  const bool has_col = ccol < D;

  gu64* e0 = (gu64*)(ws + WS_E0);
  gu64* e1 = (gu64*)(ws + WS_E1);
  gu64* e2a = (gu64*)(ws + WS_E2A);
  gu64* e2b = (gu64*)(ws + WS_E2B);
  gu64* e3a = (gu64*)(ws + WS_E3A);
  gu64* e3b = (gu64*)(ws + WS_E3B);

  STAMP(0);
  const u64 t_start = __builtin_amdgcn_s_memrealtime();
  const u64 tag = (ldg((gu64*)(ws + WS_EPOCH)) + 1ull) & 0xFFFFFFFFFFull;
  unsigned xr[2][5];
  v4u wqv[6];
  if (w < 5) {
#pragma unroll
    for (int bb = 0; bb < 2; ++bb)
#pragma unroll
      for (int i = 0; i < 5; ++i) xr[bb][i] = *(const GAS unsigned*)(x + bb * D + 2 * (lane + 64 * i));
    const unsigned short* p = wq + (size_t)(j * 80 + w * 16 + grp) * QCOLS + h * DH + sub * 8;
#pragma unroll
    for (int g = 0; g < 2; ++g)
#pragma unroll
      for (int c = 0; c < 3; ++c) wqv[g * 3 + c] = ldnt(p + (size_t)g * 8 * QCOLS + c * 64);
  }
  const size_t kvoff = ((size_t)(b * HQ + h) * SK + (SK - WIN) + s * KEYS_WG + w * 64 + grp) * DH + sub * 8;
  v4u kv[24];
#pragma unroll
  for (int g = 0; g < 2; ++g)
#pragma unroll
    for (int c = 0; c < 3; ++c) kv[g * 3 + c] = ldnt(kc + kvoff + g * 8 * DH + c * 64);
  __builtin_amdgcn_sched_barrier(0);
  STAMP(13);
  if (w < 5) {
#pragma unroll
    for (int bb = 0; bb < 2; ++bb) {
      float xv[10];
#pragma unroll
      for (int i = 0; i < 5; ++i) { xv[2 * i] = lo2f(xr[bb][i]); xv[2 * i + 1] = hi2f(xr[bb][i]); }
      float mb, sd;
      ln_stats(xv, mb, sd);
#pragma unroll
      for (int i = 0; i < 5; ++i) {
        const int e = 2 * (lane + 64 * i) - j * 80;
        if (e >= 0 && e < 80) *(f32x2*)&L.xn[w][bb][e] = f32x2{ln_apply(xv[2 * i], mb, sd), ln_apply(xv[2 * i + 1], mb, sd)};
      }
    }
    asm volatile("s_waitcnt lgkmcnt(0)" ::: "memory");
    STAMP(11);
    float qa[2][3][8];
#pragma unroll
    for (int bb = 0; bb < 2; ++bb)
#pragma unroll
      for (int c = 0; c < 3; ++c)
#pragma unroll
        for (int e = 0; e < 8; ++e) qa[bb][c][e] = 0.f;
#pragma unroll
    for (int g = 0; g < 2; ++g) {
      const float x0 = L.xn[w][0][w * 16 + g * 8 + grp], x1 = L.xn[w][1][w * 16 + g * 8 + grp];
#pragma unroll
      for (int c = 0; c < 3; ++c) { fma8(qa[0][c], x0, wqv[g * 3 + c]); fma8(qa[1][c], x1, wqv[g * 3 + c]); }
    }
#pragma unroll
    for (int bb = 0; bb < 2; ++bb)
#pragma unroll
      for (int c = 0; c < 3; ++c)
#pragma unroll
        for (int e = 0; e < 8; ++e) qa[bb][c][e] = grp_sum(qa[bb][c][e]);
    STAMP(12);
    if (grp == 0) {
      float* dst = &L.part[0][0] + w * 384;
#pragma unroll
      for (int bb = 0; bb < 2; ++bb)
#pragma unroll
        for (int c = 0; c < 3; ++c) {
          *(f32x4*)(dst + bb * DH + c * 64 + sub * 8) = f32x4{qa[bb][c][0], qa[bb][c][1], qa[bb][c][2], qa[bb][c][3]};
          *(f32x4*)(dst + bb * DH + c * 64 + sub * 8 + 4) = f32x4{qa[bb][c][4], qa[bb][c][5], qa[bb][c][6], qa[bb][c][7]};
        }
    }
  }
  wg_barrier();
  if (tid < 384) {
    const float* src = &L.part[0][0] + tid;
    putf(e0 + bid * 384 + tid, tag, (src[0] + src[384]) + (src[768] + src[1152]) + src[1536]);
  }
  STAMP(1);
  {
    gu64* src = e0 + (size_t)(peer0 + 8 * w) * 384 + b * DH + lane;
    u64 g0 = ldg(src), g1 = ldg(src + 64), g2 = ldg(src + 128);
    __builtin_amdgcn_sched_barrier(0);
#pragma unroll
    for (int g = 2; g < 8; ++g)
#pragma unroll
      for (int c = 0; c < 3; ++c) kv[g * 3 + c] = ldnt(kc + kvoff + g * 8 * DH + c * 64);
    __builtin_amdgcn_sched_barrier(0);
    for (unsigned spins = 0; !__all(okf(g0, tag) && okf(g1, tag) && okf(g2, tag)) && spins < SPIN_CAP; ++spins) {
      __builtin_amdgcn_s_sleep(1);
      g0 = ldg(src); g1 = ldg(src + 64); g2 = ldg(src + 128);
    }
    L.raw[w][lane] = valf(g0); L.raw[w][64 + lane] = valf(g1); L.raw[w][128 + lane] = valf(g2);
  }
  v4u vv[24];
#pragma unroll
  for (int g = 0; g < 8; ++g)
#pragma unroll
    for (int c = 0; c < 3; ++c) vv[g * 3 + c] = ldnt(vc + kvoff + g * 8 * DH + c * 64);
  __builtin_amdgcn_sched_barrier(0);
  wg_barrier();
  if (tid < DH) {
    float q = 0.f;
#pragma unroll
    for (int i = 0; i < 8; ++i) q += L.raw[i][tid];
    L.qs[tid] = f2bf(q);
  }
  wg_barrier();
  STAMP(2);
  v4u qp[3];
#pragma unroll
  for (int c = 0; c < 3; ++c) qp[c] = *(const v4u*)&L.qs[c * 64 + sub * 8];

  float sc[8];
#pragma unroll
  for (int g = 0; g < 8; ++g) {
    float a = 0.f;
#pragma unroll
    for (int c = 0; c < 3; ++c)
#pragma unroll
      for (int e = 0; e < 4; ++e) a = dot2(kv[g * 3 + c][e], qp[c][e], a);
    a = oct_sum(a);
    sc[g] = rbf(rbf(a) * 0.07216878364870322f);
  }
  STAMP(3);

  v4u wov[4], f1w[3], f2w;
  {
    const int r10 = lane / 10, c10 = lane - r10 * 10;
    const unsigned short* p = wo + (size_t)(h * DH + w * 24 + (lane < 60 ? r10 : 0)) * D + j * 80 + (lane < 60 ? c10 : 0) * 8;
#pragma unroll
    for (int i = 0; i < 4; ++i) wov[i] = ldnt(p + (size_t)i * 6 * D);
    const unsigned short* p1 = (m1 ? wu : wg) + (size_t)((is_f1 ? kq1 : 0) * 160 + (tid >> 3)) * FF + cb1 * 64 + sub * 8;
    f1w[0] = ldnt(p1);
    f1w[1] = ldnt(p1 + (size_t)64 * FF);
    f1w[2] = ldnt(p1 + (size_t)(tid < 256 ? 128 : 0) * FF);
    f2w = ldnt(wd + (size_t)((is_f2 ? kp2 : 0) * 64 + (tid >> 3)) * D + cb2 * 64 + sub * 8);
  }
  const float xres = has_col ? bf2f(x[(lane >> 5) * D + ccol]) : 0.f;
  __builtin_amdgcn_sched_barrier(0);

  {
    float m = sc[0];
#pragma unroll
    for (int g = 1; g < 8; ++g) m = fmaxf(m, sc[g]);
    m = grp_max(m);
    float l = 0.f, p[8];
#pragma unroll
    for (int g = 0; g < 8; ++g) { p[g] = __expf(sc[g] - m); l += p[g]; }
    l = grp_sum(l);
    float acc[3][8];
#pragma unroll
    for (int c = 0; c < 3; ++c)
#pragma unroll
      for (int e = 0; e < 8; ++e) acc[c][e] = 0.f;
#pragma unroll
    for (int g = 0; g < 8; ++g)
#pragma unroll
      for (int c = 0; c < 3; ++c) fma8(acc[c], p[g], vv[g * 3 + c]);
#pragma unroll
    for (int c = 0; c < 3; ++c)
#pragma unroll
      for (int e = 0; e < 8; ++e) acc[c][e] = grp_sum(acc[c][e]);
    if (grp == 0) {
#pragma unroll
      for (int c = 0; c < 3; ++c) {
        *(f32x4*)&L.raw[w][c * 64 + sub * 8] = f32x4{acc[c][0], acc[c][1], acc[c][2], acc[c][3]};
        *(f32x4*)&L.raw[w][c * 64 + sub * 8 + 4] = f32x4{acc[c][4], acc[c][5], acc[c][6], acc[c][7]};
      }
      if (sub == 0) { L.mw[w] = m; L.lw[w] = l; }
    }
  }
  wg_barrier();
  if (tid < 194) {
    float M = L.mw[0];
#pragma unroll
    for (int i = 1; i < 8; ++i) M = fmaxf(M, L.mw[i]);
    float o = 0.f, l = 0.f;
#pragma unroll
    for (int i = 0; i < 8; ++i) {
      const float wt = __expf(L.mw[i] - M);
      o = fmaf(wt, L.raw[i][tid < DH ? tid : 0], o);
      l = fmaf(wt, L.lw[i], l);
    }
    putf(e1 + bid * 256 + tid, tag, tid < DH ? o : (tid == DH ? M : l));
  }
  wg_barrier();
  STAMP(4);

  {
    gu64* src = e1 + (size_t)(peer0 + 8 * w) * 256 + lane;
    const int o3 = lane < 2 ? 192 : 0;
    u64 g0 = ldg(src), g1 = ldg(src + 64), g2 = ldg(src + 128), g3 = ldg(src + o3);
    for (unsigned spins = 0; !__all(okf(g0, tag) && okf(g1, tag) && okf(g2, tag) && okf(g3, tag)) && spins < SPIN_CAP; ++spins) {
      __builtin_amdgcn_s_sleep(1);
      g0 = ldg(src); g1 = ldg(src + 64); g2 = ldg(src + 128); g3 = ldg(src + o3);
    }
    L.raw[w][lane] = valf(g0); L.raw[w][64 + lane] = valf(g1); L.raw[w][128 + lane] = valf(g2);
    if (lane < 2) L.raw[w][192 + lane] = valf(g3);
  }
  wg_barrier();
  STAMP(5);
  if (tid < 2 * DH) {
    const int bb = tid >= DH ? 1 : 0, d = tid - bb * DH;
    float M = L.raw[bb * 4][192];
#pragma unroll
    for (int i = 1; i < 4; ++i) M = fmaxf(M, L.raw[bb * 4 + i][192]);
    float o = 0.f, l = 0.f;
#pragma unroll
    for (int i = 0; i < 4; ++i) {
      const float wt = __expf(L.raw[bb * 4 + i][192] - M);
      o = fmaf(wt, L.raw[bb * 4 + i][d], o);
      l = fmaf(wt, L.raw[bb * 4 + i][193], l);
    }
    L.on[bb][d] = rbf(o / l);
  }
  wg_barrier();
  if (lane < 60) {
    const int r10 = lane / 10, c10 = lane - r10 * 10;
    float a0[8], a1[8];
#pragma unroll
    for (int e = 0; e < 8; ++e) { a0[e] = 0.f; a1[e] = 0.f; }
#pragma unroll
    for (int i = 0; i < 4; ++i) {
      const int row = w * 24 + i * 6 + r10;
      fma8(a0, L.on[0][row], wov[i]);
      fma8(a1, L.on[1][row], wov[i]);
    }
    float* dst = &L.part[w * 6 + r10][c10 * 16];
#pragma unroll
    for (int e = 0; e < 8; e += 2) *(f32x4*)(dst + 2 * e) = f32x4{a0[e], a1[e], a0[e + 1], a1[e + 1]};
  }
  wg_barrier();
  if (tid < 160) {
    float t0 = 0.f, t1 = 0.f, t2 = 0.f, t3 = 0.f;
#pragma unroll
    for (int i = 0; i < 48; i += 4) { t0 += L.part[i][tid]; t1 += L.part[i + 1][tid]; t2 += L.part[i + 2][tid]; t3 += L.part[i + 3][tid]; }
    putf(e2a + (size_t)(j * 160 + tid) * 32 + h, tag, (t0 + t1) + (t2 + t3));
  }
  STAMP(6);

  float hres = 0.f;
  if (has_col) {
    gu64* src = e2a + (size_t)ccol * 64 + lane;
    u64 g0 = ldg(src);
    for (unsigned spins = 0; !__all(okf(g0, tag)) && spins < SPIN_CAP; ++spins) { __builtin_amdgcn_s_sleep(1); g0 = ldg(src); }
    hres = rbf(xres + rbf(half_sum(valf(g0))));
    const unsigned hb = f2bf(hres);
    const unsigned hi = __builtin_amdgcn_readlane(hb, 32);
    if (lane == 0) putu(e2b + ccol, tag, hb | (hi << 16));
  }
  STAMP(7);

  if (is_f1) {
    {
      const int o1 = tid < 128 ? 512 : 0;
      u64 g0 = ldg(e2b + tid), g1 = ldg(e2b + tid + o1);
      for (unsigned spins = 0; !__all(oku(g0, tag) && oku(g1, tag)) && spins < SPIN_CAP; ++spins) {
        __builtin_amdgcn_s_sleep(1);
        g0 = ldg(e2b + tid); g1 = ldg(e2b + tid + o1);
      }
      L.hv[0][tid] = lo2f((unsigned)g0); L.hv[1][tid] = hi2f((unsigned)g0);
      if (tid < 128) { L.hv[0][512 + tid] = lo2f((unsigned)g1); L.hv[1][512 + tid] = hi2f((unsigned)g1); }
    }
    wg_barrier();
    {
      float v0[10], v1[10], m0, s0, m1v, s1;
#pragma unroll
      for (int i = 0; i < 10; ++i) { v0[i] = L.hv[0][lane + 64 * i]; v1[i] = L.hv[1][lane + 64 * i]; }
      ln_stats(v0, m0, s0);
      ln_stats(v1, m1v, s1);
      float a0[8], a1[8];
#pragma unroll
      for (int e = 0; e < 8; ++e) { a0[e] = 0.f; a1[e] = 0.f; }
      const int r0 = kq1 * 160 + (tid >> 3);
#pragma unroll
      for (int i = 0; i < 3; ++i) {
        const int row = r0 + 64 * i;
        const bool live = i < 2 || tid < 256;
        const float h0 = live ? ln_apply(L.hv[0][live ? row : 0], m0, s0) : 0.f;
        const float h1 = live ? ln_apply(L.hv[1][live ? row : 0], m1v, s1) : 0.f;
        fma8(a0, h0, f1w[i]);
        fma8(a1, h1, f1w[i]);
      }
#pragma unroll
      for (int e = 0; e < 8; ++e) { a0[e] = grp_sum(a0[e]); a1[e] = grp_sum(a1[e]); }
      if (grp == 0) {
        *(f32x4*)&L.red[w][sub * 8] = f32x4{a0[0], a0[1], a0[2], a0[3]};
        *(f32x4*)&L.red[w][sub * 8 + 4] = f32x4{a0[4], a0[5], a0[6], a0[7]};
        *(f32x4*)&L.red[w][64 + sub * 8] = f32x4{a1[0], a1[1], a1[2], a1[3]};
        *(f32x4*)&L.red[w][64 + sub * 8 + 4] = f32x4{a1[4], a1[5], a1[6], a1[7]};
      }
    }
    wg_barrier();
    if (tid < 128) {
      float t = 0.f;
#pragma unroll
      for (int i = 0; i < 8; ++i) t += L.red[i][tid];
      const int n = cb1 * 64 + (tid & 63), bb = tid >> 6;
      putf(e3a + (size_t)n * 16 + (m1 * 2 + bb) * 4 + kq1, tag, t);
    }
  }
  STAMP(8);

  if (is_f2) {
    gu64* src = e3a + (size_t)(kp2 * 64 + (tid >> 3)) * 16 + sub * 2;
    u64 g0 = ldg(src), g1 = ldg(src + 1);
    for (unsigned spins = 0; !__all(okf(g0, tag) && okf(g1, tag)) && spins < SPIN_CAP; ++spins) {
      __builtin_amdgcn_s_sleep(1);
      g0 = ldg(src); g1 = ldg(src + 1);
    }
    float t = valf(g0) + valf(g1);
    t += dpp<XOR1>(t);
    const float A = dpp<QP0>(t), Bv = dpp<QP2>(t);
    const float A2 = dpp<HMIRROR>(A), B2 = dpp<HMIRROR>(Bv);
    const bool lowq = sub < 4;
    const float g_0 = rbf(lowq ? A : A2), g_1 = rbf(lowq ? Bv : B2), u_0 = rbf(lowq ? A2 : A), u_1 = rbf(lowq ? B2 : Bv);
    const float ge0 = rbf(0.5f * g_0 * (1.0f + tanhf(0.7978845608028654f * (g_0 + 0.044715f * g_0 * g_0 * g_0))));
    const float ge1 = rbf(0.5f * g_1 * (1.0f + tanhf(0.7978845608028654f * (g_1 + 0.044715f * g_1 * g_1 * g_1))));
    const float act0 = rbf(ge0 * u_0), act1 = rbf(ge1 * u_1);
    float a0[8], a1[8];
#pragma unroll
    for (int e = 0; e < 8; ++e) { a0[e] = 0.f; a1[e] = 0.f; }
    fma8(a0, act0, f2w);
    fma8(a1, act1, f2w);
#pragma unroll
    for (int e = 0; e < 8; ++e) { a0[e] = grp_sum(a0[e]); a1[e] = grp_sum(a1[e]); }
    wg_barrier();
    if (grp == 0) {
      *(f32x4*)&L.red[w][sub * 8] = f32x4{a0[0], a0[1], a0[2], a0[3]};
      *(f32x4*)&L.red[w][sub * 8 + 4] = f32x4{a0[4], a0[5], a0[6], a0[7]};
      *(f32x4*)&L.red[w][64 + sub * 8] = f32x4{a1[0], a1[1], a1[2], a1[3]};
      *(f32x4*)&L.red[w][64 + sub * 8 + 4] = f32x4{a1[4], a1[5], a1[6], a1[7]};
    }
    wg_barrier();
    if (tid < 128) {
      float t2 = 0.f;
#pragma unroll
      for (int i = 0; i < 8; ++i) t2 += L.red[i][tid];
      const int c = cb2 * 64 + (tid & 63), bb = tid >> 6;
      putf(e3b + (size_t)c * 64 + bb * 32 + kp2, tag, t2);
    }
  }
  STAMP(9);

  if (has_col) {
    const bool live = (lane & 31) < 24;
    gu64* src = e3b + (size_t)ccol * 64 + (live ? lane : (lane & 32));
    u64 g0 = ldg(src);
    for (unsigned spins = 0; !__all(okf(g0, tag)) && spins < SPIN_CAP; ++spins) { __builtin_amdgcn_s_sleep(1); g0 = ldg(src); }
    const float f = half_sum(live ? valf(g0) : 0.f);
    if ((lane & 31) == 0) out[(lane >> 5) * D + ccol] = f2bf(hres + rbf(f));
    if (ccol == 0 && lane == 0) __hip_atomic_store((gu64*)(ws + WS_EPOCH), tag, RLX_AGENT);
  }
  STAMP(10);
  if (w == 0 && DIAG) {
    const u64 tn = __builtin_amdgcn_s_memrealtime();
    const u64 t1 = tn + (tn - t_start);
    while (__builtin_amdgcn_s_memrealtime() < t1) __builtin_amdgcn_s_sleep(1);
  }
}
}

extern "C" void kernel_launch(void* const* d_in, const int* in_sizes, int n_in, void* d_out, int out_size, void* d_ws,
                              size_t ws_size, hipStream_t stream) {
  const unsigned short* x = (const unsigned short*)d_in[0];
  const unsigned short* kc = (const unsigned short*)d_in[1];
  const unsigned short* vc = (const unsigned short*)d_in[2];
  const unsigned short* wq = (const unsigned short*)d_in[3];
  const unsigned short* wo = (const unsigned short*)d_in[4];
  const unsigned short* wg = (const unsigned short*)d_in[6];
  const unsigned short* wu = (const unsigned short*)d_in[7];
  const unsigned short* wd = (const unsigned short*)d_in[8];
  fused_kernel<<<dim3(NWG), dim3(NTHR), 0, stream>>>(x, kc, vc, wq, wo, wg, wu, wd, (unsigned short*)d_out,
                                                      (unsigned char*)d_ws);
}
```

```cpp
#include <hip/hip_runtime.h>
#include <hip/hip_bf16.h>
#include <stdint.h>

#define GAS __attribute__((address_space(1)))
typedef unsigned v4u __attribute__((ext_vector_type(4)));
typedef float f32x4 __attribute__((ext_vector_type(4)));
typedef float f32x2 __attribute__((ext_vector_type(2)));
typedef __bf16 bf16x2 __attribute__((ext_vector_type(2)));
typedef unsigned long long u64;
typedef GAS u64 gu64;
#define RLX_AGENT __ATOMIC_RELAXED, __HIP_MEMORY_SCOPE_AGENT
#define RLX_WG __ATOMIC_RELAXED, __HIP_MEMORY_SCOPE_WORKGROUP

namespace {
constexpr int D = 640, HQ = 32, DH = 192, SK = 8192, WIN = 2048, FF = 1536;
constexpr int QCOLS = HQ * DH;
constexpr int NWG = 256, NTHR = 512;
constexpr int KEYS_WG = WIN / 4;
constexpr unsigned SPIN_CAP = 1u << 20;

constexpr size_t WS_EPOCH = 0;
constexpr size_t WS_E0 = 4096;
constexpr size_t WS_E1 = WS_E0 + (size_t)NWG * 384 * 8;
constexpr size_t WS_E2A = WS_E1 + (size_t)NWG * 256 * 8;
constexpr size_t WS_E2B = WS_E2A + (size_t)D * 64 * 8;
constexpr size_t WS_E3A = WS_E2B + (size_t)8 * D * 8;
constexpr size_t WS_E3B = WS_E3A + (size_t)FF * 16 * 8;

__device__ __forceinline__ float bf2f(unsigned short u) { return __uint_as_float((unsigned)u << 16); }
__device__ __forceinline__ float lo2f(unsigned u) { return __uint_as_float(u << 16); }
__device__ __forceinline__ float hi2f(unsigned u) { return __uint_as_float(u & 0xffff0000u); }
__device__ __forceinline__ unsigned short f2bf(float f) { return __builtin_bit_cast(unsigned short, (__bf16)f); }
__device__ __forceinline__ float rbf(float f) { return (float)(__bf16)f; }
__device__ __forceinline__ float dot2(unsigned k, unsigned q, float acc) {
  return __builtin_amdgcn_fdot2_f32_bf16(__builtin_bit_cast(bf16x2, k), __builtin_bit_cast(bf16x2, q), acc, false);
}
template <int CTRL> __device__ __forceinline__ float dpp(float x) {
  return __int_as_float(__builtin_amdgcn_mov_dpp(__float_as_int(x), CTRL, 0xf, 0xf, true));
}
constexpr int XOR1 = 0xB1, XOR2 = 0x4E, HMIRROR = 0x141, ROR8 = 0x128;
__device__ __forceinline__ float fold32(float a, float b) {
  auto s = __builtin_amdgcn_permlane32_swap(__float_as_uint(a), __float_as_uint(b), false, false);
  return __uint_as_float(s[0]) + __uint_as_float(s[1]);
}
__device__ __forceinline__ float fold16(float a, float b) {
  auto s = __builtin_amdgcn_permlane16_swap(__float_as_uint(a), __float_as_uint(b), false, false);
  return __uint_as_float(s[0]) + __uint_as_float(s[1]);
}
__device__ __forceinline__ float fold8(float a, float b, bool bit3) {
  const float t = a + dpp<ROR8>(a), u = b + dpp<ROR8>(b);
  return bit3 ? u : t;
}
template <int N> __device__ __forceinline__ void fold_groups(const float (&v)[N], float (&r)[N / 8], bool bit3) {
  float a[N / 2], c[N / 4];
#pragma unroll
  for (int i = 0; i < N / 2; ++i) a[i] = fold32(v[i], v[i + N / 2]);
#pragma unroll
  for (int i = 0; i < N / 4; ++i) c[i] = fold16(a[i], a[i + N / 4]);
#pragma unroll
  for (int i = 0; i < N / 8; ++i) r[i] = fold8(c[i], c[i + N / 8], bit3);
}
__device__ __forceinline__ float swap16_max(float x) {
  auto s = __builtin_amdgcn_permlane16_swap(__float_as_uint(x), __float_as_uint(x), false, false);
  return fmaxf(__uint_as_float(s[0]), __uint_as_float(s[1]));
}
__device__ __forceinline__ float swap32_max(float x) {
  auto s = __builtin_amdgcn_permlane32_swap(__float_as_uint(x), __float_as_uint(x), false, false);
  return fmaxf(__uint_as_float(s[0]), __uint_as_float(s[1]));
}
__device__ __forceinline__ float grp_sum(float x) { x += dpp<ROR8>(x); x = fold16(x, x); return fold32(x, x); }
__device__ __forceinline__ float grp_max(float x) { x = fmaxf(x, dpp<ROR8>(x)); x = swap16_max(x); return swap32_max(x); }
__device__ __forceinline__ float oct_sum(float x) { x += dpp<XOR1>(x); x += dpp<XOR2>(x); x += dpp<HMIRROR>(x); return x; }
__device__ __forceinline__ float wave_sum(float x) { return grp_sum(oct_sum(x)); }
__device__ __forceinline__ float half_sum(float x) { x = oct_sum(x); x += dpp<ROR8>(x); return fold16(x, x); }

__device__ __forceinline__ void wg_barrier() {
  asm volatile("s_waitcnt lgkmcnt(0)" ::: "memory");
  __builtin_amdgcn_s_barrier();
  asm volatile("" ::: "memory");
}
__device__ __forceinline__ void lds_wait() { asm volatile("s_waitcnt lgkmcnt(0)" ::: "memory"); }
__device__ __forceinline__ unsigned lds_flag(unsigned* p) { return __builtin_amdgcn_readfirstlane(__hip_atomic_load(p, RLX_WG)); }

__device__ __forceinline__ void putf(gu64* g, u64 tag, float v) {
  __hip_atomic_store(g, (tag << 24) | (u64)(__float_as_uint(v) >> 8), RLX_AGENT);
}
__device__ __forceinline__ u64 ldg(gu64* g) { return __hip_atomic_load(g, RLX_AGENT); }
__device__ __forceinline__ bool okf(u64 x, u64 tag) { return (x >> 24) == tag; }
__device__ __forceinline__ float valf(u64 x) { return __uint_as_float(((unsigned)x) << 8); }
__device__ __forceinline__ void putu(gu64* g, u64 tag, unsigned v) { __hip_atomic_store(g, (tag << 32) | (u64)v, RLX_AGENT); }
__device__ __forceinline__ bool oku(u64 x, u64 tag) { return (unsigned)(x >> 32) == (unsigned)tag; }
__device__ __forceinline__ void wait_f(gu64* g, u64 tag) {
  for (unsigned spins = 0; spins < SPIN_CAP; ++spins) { if (okf(ldg(g), tag)) break; __builtin_amdgcn_s_sleep(2); }
}
__device__ __forceinline__ void wait_u(gu64* g, u64 tag) {
  for (unsigned spins = 0; spins < SPIN_CAP; ++spins) { if (oku(ldg(g), tag)) break; __builtin_amdgcn_s_sleep(2); }
}
__device__ __forceinline__ v4u ldnt(const void* p) { return __builtin_nontemporal_load((const GAS v4u*)p); }

__device__ __forceinline__ void ln_stats_exact(const float (&v)[10], float& mean_bf, float& sd) {
  float s = 0.f;
#pragma unroll
  for (int i = 0; i < 10; ++i) s += v[i];
  const float mean = wave_sum(s) / (float)D;
  float q = 0.f;
#pragma unroll
  for (int i = 0; i < 10; ++i) { const float d = v[i] - mean; q = fmaf(d, d, q); }
  const float var = wave_sum(q) / (float)D;
  mean_bf = rbf(mean);
  sd = rbf(sqrtf(rbf(rbf(var) + 1e-5f)));
}
__device__ __forceinline__ float ln_apply_exact(float x, float mean_bf, float sd) { return rbf(rbf(x - mean_bf) / sd); }
__device__ __forceinline__ float ln_apply(float x, float mean_bf, float rsd) { return rbf(rbf(x - mean_bf) * rsd); }
__device__ __forceinline__ void fma8(float* a, float s, v4u t) {
#pragma unroll
  for (int e = 0; e < 4; ++e) { a[2 * e] = fmaf(s, lo2f(t[e]), a[2 * e]); a[2 * e + 1] = fmaf(s, hi2f(t[e]), a[2 * e + 1]); }
}
__device__ __forceinline__ float gelu_tanh(float g) {
  const float y = 0.7978845608028654f * (g + 0.044715f * g * g * g);
  const float t = 1.0f - 2.0f * __builtin_amdgcn_rcpf(__expf(2.0f * y) + 1.0f);
  return 0.5f * g * (1.0f + t);
}

#define STAMP(i) do { } while (0)

struct Lds {
  float xn[5][2][80];
  float part[48][160];
  float raw[8][200];
  float raw2[8][200];
  float on[2][DH];
  float hv[2][D];
  float mw[8], lw[8];
  float red[8][128];
  unsigned short qs[DH];
  unsigned pcnt, qcnt, qflag;
};

__global__ void __launch_bounds__(NTHR, 2)
fused_kernel(const unsigned short* __restrict__ x, const unsigned short* __restrict__ kc, const unsigned short* __restrict__ vc,
             const unsigned short* __restrict__ wq, const unsigned short* __restrict__ wo, const unsigned short* __restrict__ wg,
             const unsigned short* __restrict__ wu, const unsigned short* __restrict__ wd, unsigned short* __restrict__ out,
             unsigned char* ws) {
  __shared__ __attribute__((aligned(16))) Lds L;
  const int tid = threadIdx.x, lane = tid & 63, w = __builtin_amdgcn_readfirstlane(tid >> 6);
  const int bid = blockIdx.x, xcd = bid & 7, idx = bid >> 3;
  const int h = xcd * 4 + (idx >> 3), j = idx & 7, b = j >> 2, s = j & 3;
  const int grp = lane >> 3, sub = lane & 7;
  const bool bit3 = (lane & 8) != 0;
  const int peer0 = xcd | ((idx & ~7) << 3);
  const bool is_f1 = bid < 192;
  const int cb1 = bid % 24, m1 = (bid / 24) & 1, kq1 = bid / 48;
  const bool is_f2 = bid < 240;
  const int cb2 = bid % 10, kp2 = bid / 10;
  const bool has_col = bid >= 240 && w < 5;
  const int ccol = (has_col ? (bid - 240) * 5 + w : 0) * 8;

  gu64* e0 = (gu64*)(ws + WS_E0);
  gu64* e1 = (gu64*)(ws + WS_E1);
  gu64* e2a = (gu64*)(ws + WS_E2A);
  gu64* e2b = (gu64*)(ws + WS_E2B);
  gu64* e3a = (gu64*)(ws + WS_E3A);
  gu64* e3b = (gu64*)(ws + WS_E3B);

  STAMP(0);
  const u64 tag = (ldg((gu64*)(ws + WS_EPOCH)) + 1ull) & 0xFFFFFFFFFFull;
  const unsigned short xres_raw = x[(lane & 1) * D + ccol + ((lane >> 1) & 7)];
  unsigned xr[2][5];
  v4u wqv[6];
  if (w < 5) {
#pragma unroll
    for (int bb = 0; bb < 2; ++bb)
#pragma unroll
      for (int i = 0; i < 5; ++i) xr[bb][i] = *(const GAS unsigned*)(x + bb * D + 2 * (lane + 64 * i));
    const unsigned short* p = wq + (size_t)(j * 80 + w * 16 + grp) * QCOLS + h * DH + sub * 8;
#pragma unroll
    for (int g = 0; g < 2; ++g)
#pragma unroll
      for (int c = 0; c < 3; ++c) wqv[g * 3 + c] = ldnt(p + (size_t)g * 8 * QCOLS + c * 64);
  }
  if (tid == 0) { L.pcnt = 0u; L.qcnt = 0u; L.qflag = 0u; }
  __builtin_amdgcn_sched_barrier(0);
  wg_barrier();
  const size_t kvoff = ((size_t)(b * HQ + h) * SK + (SK - WIN) + s * KEYS_WG + w * 64 + grp) * DH + sub * 8;
  v4u kv[24], vv[24];
  {
    const bool poller = w >= 1 && w <= 3;
    const int p0 = (w - 1) * 3, np = w == 3 ? 2 : 3;
    gu64* src = e0 + (size_t)(peer0 + 8 * (poller ? p0 : 0)) * 384 + b * DH + lane;
    u64 g[3][3];
    bool released = false;
#pragma unroll
    for (int bt = 0; bt < 8; ++bt) {
      if (w < 5) {
        if (bt == 1) { while (lds_flag(&L.pcnt) < 100u) __builtin_amdgcn_s_sleep(1); }
      } else if (bt >= 3 && !released) {
        asm volatile("s_waitcnt vmcnt(12)" ::: "memory");
        released = lds_flag(&L.pcnt) >= 100u;
      }
      if (bt == 5 && poller) {
#pragma unroll
        for (int i = 0; i < 3; ++i)
#pragma unroll
          for (int k = 0; k < 3; ++k) g[i][k] = ldg(src + (size_t)(i < np ? i : 0) * 8 * 384 + 64 * k);
      }
      __builtin_amdgcn_sched_barrier(0);
#pragma unroll
      for (int gg = 0; gg < 2; ++gg)
#pragma unroll
        for (int c = 0; c < 3; ++c) {
          if (bt < 4) kv[(bt * 2 + gg) * 3 + c] = ldnt(kc + kvoff + (bt * 2 + gg) * 8 * DH + c * 64);
          else vv[((bt - 4) * 2 + gg) * 3 + c] = ldnt(vc + kvoff + ((bt - 4) * 2 + gg) * 8 * DH + c * 64);
        }
      __builtin_amdgcn_sched_barrier(0);
      if (bt == 0 && w < 5) {
#pragma unroll
        for (int bb = 0; bb < 2; ++bb) {
          float xv[10];
#pragma unroll
          for (int i = 0; i < 5; ++i) { xv[2 * i] = lo2f(xr[bb][i]); xv[2 * i + 1] = hi2f(xr[bb][i]); }
          float mb, sd;
          ln_stats_exact(xv, mb, sd);
#pragma unroll
          for (int i = 0; i < 5; ++i) {
            const int e = 2 * (lane + 64 * i) - j * 80;
            if (e >= 0 && e < 80) *(f32x2*)&L.xn[w][bb][e] = f32x2{ln_apply_exact(xv[2 * i], mb, sd), ln_apply_exact(xv[2 * i + 1], mb, sd)};
          }
        }
        lds_wait();
        float qa[48], qr[6];
#pragma unroll
        for (int i = 0; i < 48; ++i) qa[i] = 0.f;
#pragma unroll
        for (int gq = 0; gq < 2; ++gq) {
          const float x0 = L.xn[w][0][w * 16 + gq * 8 + grp], x1 = L.xn[w][1][w * 16 + gq * 8 + grp];
#pragma unroll
          for (int c = 0; c < 3; ++c) { fma8(qa + c * 8, x0, wqv[gq * 3 + c]); fma8(qa + 24 + c * 8, x1, wqv[gq * 3 + c]); }
        }
        fold_groups<48>(qa, qr, bit3);
        {
          const int i0 = ((lane >> 4) & 1) * 12 + (bit3 ? 6 : 0);
          float* dst = &L.part[0][0] + w * 384 + (lane >> 5) * DH + sub * 8;
#pragma unroll
          for (int k = 0; k < 6; ++k) dst[((i0 + k) >> 3) * 64 + ((i0 + k) & 7)] = qr[k];
        }
        lds_wait();
        unsigned old = 0;
        if (lane == 0) old = __hip_atomic_fetch_add(&L.pcnt, 1u, RLX_WG);
        if (__builtin_amdgcn_readfirstlane(old) == 4u) {
#pragma unroll
          for (int k = 0; k < 6; ++k) {
            const float* sp = &L.part[0][0] + lane + 64 * k;
            putu(e0 + bid * 384 + lane + 64 * k, tag, __float_as_uint((sp[0] + sp[384]) + (sp[768] + sp[1152]) + sp[1536]));
          }
          __builtin_amdgcn_sched_barrier(0);
          if (lane == 0) __hip_atomic_store(&L.pcnt, 100u, RLX_WG);
        }
        STAMP(1);
      }
    }
    if (poller) {
      bool ok = true;
#pragma unroll
      for (int i = 0; i < 3; ++i)
#pragma unroll
        for (int k = 0; k < 3; ++k) ok = ok && oku(g[i][k], tag);
      if (__all(ok)) {
#pragma unroll
        for (int i = 0; i < 3; ++i)
          if (i < np) {
#pragma unroll
            for (int k = 0; k < 3; ++k) L.raw[p0 + i][64 * k + lane] = __uint_as_float((unsigned)g[i][k]);
          }
      } else {
        for (int i = 0; i < np; ++i) {
          gu64* sp = src + (size_t)i * 8 * 384;
          wait_u(sp - lane, tag);
          for (unsigned spins = 0; spins < SPIN_CAP; ++spins) {
            const u64 r0 = ldg(sp), r1 = ldg(sp + 64), r2 = ldg(sp + 128);
            L.raw[p0 + i][lane] = __uint_as_float((unsigned)r0);
            L.raw[p0 + i][64 + lane] = __uint_as_float((unsigned)r1);
            L.raw[p0 + i][128 + lane] = __uint_as_float((unsigned)r2);
            if (__all(oku(r0, tag) && oku(r1, tag) && oku(r2, tag))) break;
            __builtin_amdgcn_s_sleep(2);
          }
        }
      }
      lds_wait();
      unsigned old = 0;
      if (lane == 0) old = __hip_atomic_fetch_add(&L.qcnt, 1u, RLX_WG);
      if (__builtin_amdgcn_readfirstlane(old) == 2u) {
#pragma unroll
        for (int k = 0; k < 3; ++k) {
          float q = 0.f;
#pragma unroll
          for (int i = 0; i < 8; ++i) q += L.raw[i][lane + 64 * k];
          L.qs[lane + 64 * k] = f2bf(q);
        }
        lds_wait();
        if (lane == 0) __hip_atomic_store(&L.qflag, 1u, RLX_WG);
      }
    }
    while (lds_flag(&L.qflag) == 0u) __builtin_amdgcn_s_sleep(1);
    asm volatile("" ::: "memory");
  }
  STAMP(2);
  v4u qp[3];
#pragma unroll
  for (int c = 0; c < 3; ++c) qp[c] = *(const v4u*)&L.qs[c * 64 + sub * 8];

  float sc[8];
  {
#pragma unroll
    for (int g = 0; g < 8; ++g) {
      float a = 0.f;
#pragma unroll
      for (int c = 0; c < 3; ++c)
#pragma unroll
        for (int e = 0; e < 4; ++e) a = dot2(kv[g * 3 + c][e], qp[c][e], a);
      a = oct_sum(a);
      sc[g] = rbf(rbf(a) * 0.07216878364870322f);
    }
    float m = sc[0];
#pragma unroll
    for (int g = 1; g < 8; ++g) m = fmaxf(m, sc[g]);
    m = grp_max(m);
    if (lane == 0) L.mw[w] = m;
  }
  asm volatile("" :: "v"(tag));
  STAMP(3);
  wg_barrier();

  v4u wov[4], f1w[3], f2w;
  float M;
  {
    const f32x4 ma = *(const f32x4*)&L.mw[0], mb = *(const f32x4*)&L.mw[4];
    M = fmaxf(fmaxf(fmaxf(ma[0], ma[1]), fmaxf(ma[2], ma[3])), fmaxf(fmaxf(mb[0], mb[1]), fmaxf(mb[2], mb[3])));
    float l = 0.f, p[8];
#pragma unroll
    for (int g = 0; g < 8; ++g) { p[g] = __expf(sc[g] - M); l += p[g]; }
    l = grp_sum(l);
    if (lane == 0) L.lw[w] = l;
    float acc[24], r[3];
#pragma unroll
    for (int i = 0; i < 24; ++i) acc[i] = 0.f;
#pragma unroll
    for (int g = 0; g < 8; ++g) {
#pragma unroll
      for (int c = 0; c < 3; ++c) fma8(acc + c * 8, p[g], vv[g * 3 + c]);
      if (g == 4) {
        __builtin_amdgcn_sched_barrier(0);
        const int r10 = lane / 10, c10 = lane - r10 * 10;
        const unsigned short* pw = wo + (size_t)(h * DH + w * 24 + (lane < 60 ? r10 : 0)) * D + j * 80 + (lane < 60 ? c10 : 0) * 8;
#pragma unroll
        for (int i = 0; i < 4; ++i) wov[i] = ldnt(pw + (size_t)i * 6 * D);
        __builtin_amdgcn_sched_barrier(0);
      }
    }
    fold_groups<24>(acc, r, bit3);
    const int i0 = (lane >> 5) * 12 + ((lane >> 4) & 1) * 6 + (bit3 ? 3 : 0);
#pragma unroll
    for (int k = 0; k < 3; ++k) L.raw[w][((i0 + k) >> 3) * 64 + sub * 8 + ((i0 + k) & 7)] = r[k];
  }
  wg_barrier();
  if (tid < 194) {
    float o = 0.f;
#pragma unroll
    for (int i = 0; i < 8; ++i) o += tid < DH ? L.raw[i][tid] : L.lw[i];
    putf(e1 + bid * 256 + tid, tag, tid == DH ? M : o);
  }
  STAMP(4);

  {
    gu64* src = e1 + (size_t)(peer0 + 8 * w) * 256 + lane;
    const int o3 = lane < 2 ? 192 : 0;
    u64 g0 = ldg(src), g1 = ldg(src + 64), g2 = ldg(src + 128), g3 = ldg(src + o3);
    if (!__all(okf(g0, tag) && okf(g1, tag) && okf(g2, tag) && okf(g3, tag))) {
      wait_f(src - lane + 193, tag);
      for (unsigned spins = 0; spins < SPIN_CAP; ++spins) {
        g0 = ldg(src); g1 = ldg(src + 64); g2 = ldg(src + 128); g3 = ldg(src + o3);
        if (__all(okf(g0, tag) && okf(g1, tag) && okf(g2, tag) && okf(g3, tag))) break;
        __builtin_amdgcn_s_sleep(1);
      }
    }
    STAMP(12);
    L.raw2[w][lane] = valf(g0); L.raw2[w][64 + lane] = valf(g1); L.raw2[w][128 + lane] = valf(g2);
    if (lane < 2) L.raw2[w][192 + lane] = valf(g3);
  }
  wg_barrier();
  STAMP(5);
  if (lane < 48) {
    const int bb = lane >= 24 ? 1 : 0, d = w * 24 + lane - bb * 24;
    const float m0 = L.raw2[bb * 4][192], m1v = L.raw2[bb * 4 + 1][192], m2 = L.raw2[bb * 4 + 2][192], m3 = L.raw2[bb * 4 + 3][192];
    const float MM = fmaxf(fmaxf(m0, m1v), fmaxf(m2, m3));
    const float w0 = __expf(m0 - MM), w1 = __expf(m1v - MM), w2 = __expf(m2 - MM), w3 = __expf(m3 - MM);
    const float o = (w0 * L.raw2[bb * 4][d] + w1 * L.raw2[bb * 4 + 1][d]) + (w2 * L.raw2[bb * 4 + 2][d] + w3 * L.raw2[bb * 4 + 3][d]);
    const float l = (w0 * L.raw2[bb * 4][193] + w1 * L.raw2[bb * 4 + 1][193]) + (w2 * L.raw2[bb * 4 + 2][193] + w3 * L.raw2[bb * 4 + 3][193]);
    L.on[bb][d] = rbf(o * __builtin_amdgcn_rcpf(l));
  }
  lds_wait();
  if (lane < 60) {
    const int r10 = lane / 10, c10 = lane - r10 * 10;
    float a0[8], a1[8];
#pragma unroll
    for (int e = 0; e < 8; ++e) { a0[e] = 0.f; a1[e] = 0.f; }
#pragma unroll
    for (int i = 0; i < 4; ++i) {
      const int row = w * 24 + i * 6 + r10;
      fma8(a0, L.on[0][row], wov[i]);
      fma8(a1, L.on[1][row], wov[i]);
    }
    float* dst = &L.part[w * 6 + r10][c10 * 16];
#pragma unroll
    for (int e = 0; e < 8; e += 2) *(f32x4*)(dst + 2 * e) = f32x4{a0[e], a1[e], a0[e + 1], a1[e + 1]};
  }
  wg_barrier();
  if (tid < 320) {
    const int o = tid >> 1, hf = (tid & 1) * 24;
    float t0 = 0.f, t1 = 0.f, t2 = 0.f, t3 = 0.f;
#pragma unroll
    for (int i = 0; i < 24; i += 4) { t0 += L.part[hf + i][o]; t1 += L.part[hf + i + 1][o]; t2 += L.part[hf + i + 2][o]; t3 += L.part[hf + i + 3][o]; }
    float t = (t0 + t1) + (t2 + t3);
    t += dpp<XOR1>(t);
    if ((tid & 1) == 0) putf(e2a + (size_t)h * (2 * D) + j * 160 + o, tag, t);
  }
  if (is_f2) {
    const unsigned short* p1 = (m1 ? wu : wg) + (size_t)((is_f1 ? kq1 : 0) * 160 + (tid >> 3)) * FF + cb1 * 64 + sub * 8;
    f1w[0] = ldnt(p1);
    f1w[1] = ldnt(p1 + (size_t)64 * FF);
    f1w[2] = ldnt(p1 + (size_t)(tid < 256 ? 128 : 0) * FF);
    f2w = ldnt(wd + (size_t)(kp2 * 64 + (tid >> 3)) * D + cb2 * 64 + sub * 8);
  }
  __builtin_amdgcn_sched_barrier(0);
  STAMP(6);

  float hres = 0.f;
  if (has_col) {
    gu64* src = e2a + (size_t)(lane >> 4) * (2 * D) + ccol * 2 + (lane & 15);
    u64 g[8];
    for (unsigned spins = 0; spins < SPIN_CAP; ++spins) {
      bool ok = true;
#pragma unroll
      for (int i = 0; i < 8; ++i) g[i] = ldg(src + (size_t)i * 4 * (2 * D));
#pragma unroll
      for (int i = 0; i < 8; ++i) ok = ok && okf(g[i], tag);
      if (__all(ok)) break;
      __builtin_amdgcn_s_sleep(1);
    }
    float t = ((valf(g[0]) + valf(g[1])) + (valf(g[2]) + valf(g[3]))) + ((valf(g[4]) + valf(g[5])) + (valf(g[6]) + valf(g[7])));
    t = fold16(t, t);
    t = fold32(t, t);
    hres = rbf(bf2f(xres_raw) + rbf(t));
    const unsigned hb = f2bf(hres);
    const unsigned other = __builtin_amdgcn_mov_dpp(hb, XOR1, 0xf, 0xf, true);
    if ((lane & 1) == 0) {
      putu(e2b + (lane >> 4) * D + ccol + ((lane >> 1) & 7), tag, hb | (other << 16));
      putu(e2b + ((lane >> 4) + 4) * D + ccol + ((lane >> 1) & 7), tag, hb | (other << 16));
    }
    STAMP(14);
  }

  if (is_f1) {
    {
      const int o1 = tid < 128 ? 512 : 0;
      gu64* src = e2b + xcd * D + tid;
      wait_u(src - lane, tag);
      u64 g0 = ldg(src), g1 = ldg(src + o1);
      for (unsigned spins = 0; !__all(oku(g0, tag) && oku(g1, tag)) && spins < SPIN_CAP; ++spins) {
        __builtin_amdgcn_s_sleep(1);
        g0 = ldg(src); g1 = ldg(src + o1);
      }
      STAMP(16);
      L.hv[0][tid] = lo2f((unsigned)g0); L.hv[1][tid] = hi2f((unsigned)g0);
      if (tid < 128) { L.hv[0][512 + tid] = lo2f((unsigned)g1); L.hv[1][512 + tid] = hi2f((unsigned)g1); }
    }
    wg_barrier();
    {
      float m0, s0, m1v, s1;
      const int r0 = kq1 * 160 + (tid >> 3);
      float hr0[3], hr1[3];
      {
        f32x4 v[5];
#pragma unroll
        for (int i = 0; i < 5; ++i) v[i] = *(const f32x4*)&L.hv[lane >> 5][(lane & 31) * 4 + 128 * i];
#pragma unroll
        for (int i = 0; i < 3; ++i) { const int row = (i < 2 || tid < 256) ? r0 + 64 * i : 0; hr0[i] = L.hv[0][row]; hr1[i] = L.hv[1][row]; }
        const f32x4 sv = (v[0] + v[1]) + (v[2] + v[3]) + v[4];
        const float mean = half_sum((sv[0] + sv[1]) + (sv[2] + sv[3])) * (1.0f / D);
        f32x4 qv = {0.f, 0.f, 0.f, 0.f};
#pragma unroll
        for (int i = 0; i < 5; ++i) { const f32x4 d = v[i] - mean; qv += d * d; }
        const float var = half_sum((qv[0] + qv[1]) + (qv[2] + qv[3])) * (1.0f / D);
        const float mb = rbf(mean), rs = __builtin_amdgcn_rcpf(rbf(__builtin_amdgcn_sqrtf(rbf(rbf(var) + 1e-5f))));
        auto pm = __builtin_amdgcn_permlane32_swap(__float_as_uint(mb), __float_as_uint(mb), false, false);
        auto pr = __builtin_amdgcn_permlane32_swap(__float_as_uint(rs), __float_as_uint(rs), false, false);
        m0 = __uint_as_float(pm[0]); m1v = __uint_as_float(pm[1]); s0 = __uint_as_float(pr[0]); s1 = __uint_as_float(pr[1]);
      }
      float a[16], r[2];
#pragma unroll
      for (int e = 0; e < 16; ++e) a[e] = 0.f;
#pragma unroll
      for (int i = 0; i < 3; ++i) {
        const bool live = i < 2 || tid < 256;
        fma8(a, live ? ln_apply(hr0[i], m0, s0) : 0.f, f1w[i]);
        fma8(a + 8, live ? ln_apply(hr1[i], m1v, s1) : 0.f, f1w[i]);
      }
      fold_groups<16>(a, r, bit3);
      *(f32x2*)&L.red[w][(lane >> 5) * 64 + sub * 8 + ((lane >> 4) & 1) * 4 + (bit3 ? 2 : 0)] = f32x2{r[0], r[1]};
    }
    wg_barrier();
    if (tid < 128) {
      float t = 0.f;
#pragma unroll
      for (int i = 0; i < 8; ++i) t += L.red[i][tid];
      putf(e3a + (size_t)((cb1 * 2 + m1) * 4 + kq1) * 128 + (tid & 63) * 2 + (tid >> 6), tag, t);
    }
  }
  STAMP(8);

  if (is_f2) {
    gu64* src = e3a + (size_t)(kp2 * 8 + sub) * 128 + (tid >> 3) * 2;
    if (w == 0) {
      gu64* sent = e3a + (size_t)(kp2 * 8 + sub) * 128 + (grp & 1) * 127;
      for (unsigned spins = 0; spins < SPIN_CAP; ++spins) { if (__all(okf(ldg(sent), tag))) break; __builtin_amdgcn_s_sleep(1); }
    }
    wg_barrier();
    u64 g0 = ldg(src), g1 = ldg(src + 1);
    for (unsigned spins = 0; !__all(okf(g0, tag) && okf(g1, tag)) && spins < SPIN_CAP; ++spins) {
      __builtin_amdgcn_s_sleep(1);
      g0 = ldg(src); g1 = ldg(src + 1);
    }
    STAMP(20);
    float t0 = valf(g0), t1 = valf(g1);
    t0 += dpp<XOR1>(t0); t1 += dpp<XOR1>(t1);
    t0 += dpp<XOR2>(t0); t1 += dpp<XOR2>(t1);
    const float o0 = dpp<HMIRROR>(t0), o1 = dpp<HMIRROR>(t1);
    const bool lowq = sub < 4;
    const float g_0 = rbf(lowq ? t0 : o0), g_1 = rbf(lowq ? t1 : o1), u_0 = rbf(lowq ? o0 : t0), u_1 = rbf(lowq ? o1 : t1);
    const float act0 = rbf(rbf(gelu_tanh(g_0)) * u_0), act1 = rbf(rbf(gelu_tanh(g_1)) * u_1);
    float a[16], r[2];
#pragma unroll
    for (int e = 0; e < 16; ++e) a[e] = 0.f;
    fma8(a, act0, f2w);
    fma8(a + 8, act1, f2w);
    fold_groups<16>(a, r, bit3);
    *(f32x2*)&L.part[w][(lane >> 5) * 64 + sub * 8 + ((lane >> 4) & 1) * 4 + (bit3 ? 2 : 0)] = f32x2{r[0], r[1]};
    wg_barrier();
    if (tid < 128) {
      float t2 = 0.f;
#pragma unroll
      for (int i = 0; i < 8; ++i) t2 += L.part[i][tid];
      putf(e3b + (size_t)kp2 * (2 * D) + (cb2 * 64 + (tid & 63)) * 2 + (tid >> 6), tag, t2);
    }
  }
  STAMP(9);

  if (has_col) {
    gu64* src = e3b + (size_t)(lane >> 4) * (2 * D) + ccol * 2 + (lane & 15);
    u64 g[6];
    for (unsigned spins = 0; spins < SPIN_CAP; ++spins) {
      bool ok = true;
#pragma unroll
      for (int i = 0; i < 6; ++i) g[i] = ldg(src + (size_t)i * 4 * (2 * D));
#pragma unroll
      for (int i = 0; i < 6; ++i) ok = ok && okf(g[i], tag);
      if (__all(ok)) break;
      __builtin_amdgcn_s_sleep(1);
    }
    float t = ((valf(g[0]) + valf(g[1])) + (valf(g[2]) + valf(g[3]))) + (valf(g[4]) + valf(g[5]));
    t = fold16(t, t);
    t = fold32(t, t);
    if (lane < 16) out[(lane & 1) * D + ccol + (lane >> 1)] = f2bf(hres + rbf(t));
    if (ccol == 0 && lane == 0) __hip_atomic_store((gu64*)(ws + WS_EPOCH), tag, RLX_AGENT);
  }
  STAMP(10);
}
}

extern "C" void kernel_launch(void* const* d_in, const int* in_sizes, int n_in, void* d_out, int out_size, void* d_ws,
                              size_t ws_size, hipStream_t stream) {
  const unsigned short* x = (const unsigned short*)d_in[0];
  const unsigned short* kc = (const unsigned short*)d_in[1];
  const unsigned short* vc = (const unsigned short*)d_in[2];
  const unsigned short* wq = (const unsigned short*)d_in[3];
  const unsigned short* wo = (const unsigned short*)d_in[4];
  const unsigned short* wg = (const unsigned short*)d_in[6];
  const unsigned short* wu = (const unsigned short*)d_in[7];
  const unsigned short* wd = (const unsigned short*)d_in[8];
  fused_kernel<<<dim3(NWG), dim3(NTHR), 0, stream>>>(x, kc, vc, wq, wo, wg, wu, wd, (unsigned short*)d_out,
                                                      (unsigned char*)d_ws);
}
```
